```python
import torch

def _softmax(x, axis=-1):
    return torch.softmax(x, dim=axis)
def _gelu(x):
    return torch.nn.functional.gelu(x, approximate="tanh")
def _silu(x):
    return torch.nn.functional.silu(x)
def _sigmoid(x):
    return torch.sigmoid(x)
def _layernorm(x, axis=-1, eps=1e-5):
    m = x.mean(dim=axis, keepdim=True)
    v = x.var(dim=axis, keepdim=True, unbiased=False)
    return (x - m) / torch.sqrt(v + eps)
def _scatter_add(tgt, idx, src, axis=0):
    return tgt.clone().index_add_(axis, idx, src)
def _segment_sum(src, seg, n):
    out = torch.zeros((n,) + src.shape[1:], dtype=src.dtype)
    return out.index_add_(0, seg, src)
def _pad(x, pw):
    flat = tuple(v for pair in reversed(pw) for v in pair)
    return torch.nn.functional.pad(x, flat)

kernel_name = 'interleave_layers__sliding_block__geglu_ffn__prompt'

def setup_inputs():

    _w = (lambda a: (lambda t: t.to(torch.bfloat16) if t.is_floating_point() else t)(torch.as_tensor(a)))
    def _A_setup():
        B, S, D = 1, 2048, 448
        return {
            "x": _w((lambda *s: torch.randn(*s, dtype=torch.bfloat16) * 0.05)(B, S, D)),
            "wqkv": _w((lambda *s: torch.randn(*s, dtype=torch.bfloat16) * 0.05)(D, 3 * D)),
            "wo": _w((lambda *s: torch.randn(*s, dtype=torch.bfloat16) * 0.05)(D, D)),
        }
    _A_in = _A_setup()

    def _B_setup():
        B, S, D, F = 1, 2048, 448, 1792
        return {
            "x": _w((lambda *s: torch.randn(*s, dtype=torch.bfloat16) * 0.05)(B, S, D)),
            "w_gate": _w((lambda *s: torch.randn(*s, dtype=torch.bfloat16) * 0.05)(D, F)),
            "w_up": _w((lambda *s: torch.randn(*s, dtype=torch.bfloat16) * 0.05)(D, F)),
            "w_down": _w((lambda *s: torch.randn(*s, dtype=torch.bfloat16) * 0.05)(F, D)),
        }
    _B_in = _B_setup()

    return {
        "A_x": _A_in["x"],
        "A_wqkv": _A_in["wqkv"],
        "A_wo": _A_in["wo"],
        "B_x": _B_in["x"],
        "B_w_gate": _B_in["w_gate"],
        "B_w_up": _B_in["w_up"],
        "B_w_down": _B_in["w_down"],
    }

def reference(A_x, A_wqkv, A_wo, B_x, B_w_gate, B_w_up, B_w_down):
    def _A_ref(x, wqkv, wo):
        B, S, D = x.shape
        H = 16; Dh = D // H; W = 256
        qkv = (lambda x, *d: x.permute(*d))((x @ wqkv).reshape(B, S, 3, H, Dh), 2, 0, 3, 1, 4)
        q, k, v = qkv[0], qkv[1], qkv[2]
        scores = torch.einsum("bhqd,bhkd->bhqk", q, k) * (Dh ** -0.5)
        idx = torch.arange(S)
        mask = (idx[:, None] - idx[None, :] >= 0) & (idx[:, None] - idx[None, :] < W)
        scores = torch.where(mask[None, None], scores, -1e9)
        p = _softmax(scores, axis=-1)
        o = torch.einsum("bhqk,bhkd->bhqd", p, v)
        return (lambda x, *d: x.permute(*d))(o, 0, 2, 1, 3).reshape(B, S, D) @ wo

    def _B_ref(x, w_gate, w_up, w_down):
        return (_gelu(x @ w_gate) * (x @ w_up)) @ w_down

    x = A_x
    x = x + _A_ref(_layernorm(x, axis=-1), A_wqkv, A_wo)
    x = x + _B_ref(_layernorm(x, axis=-1), B_w_gate, B_w_up, B_w_down)
    x = x + _A_ref(_layernorm(x, axis=-1), A_wqkv, A_wo)
    x = x + _B_ref(_layernorm(x, axis=-1), B_w_gate, B_w_up, B_w_down)
    return x
```

```cpp
#include <hip/hip_runtime.h>
#include <hip/hip_bf16.h>
typedef unsigned short u16;
__device__ __forceinline__ float bf2f(u16 v){ return __uint_as_float(((unsigned)v)<<16); }
__device__ __forceinline__ u16 f2bf(float f){ unsigned u=__float_as_uint(f); u += 0x7fffu + ((u>>16)&1u); return (u16)(u>>16); }
constexpr int S=2048, D=448, H=16, DH=28, W=256, F=1792;

__global__ void k_ln(const u16* x, u16* y){
  int row=blockIdx.x; int t=threadIdx.x;
  float v[7]; float s=0;
  for(int i=0;i<7;i++){ v[i]=bf2f(x[row*D+t+64*i]); s+=v[i]; }
  for(int o=32;o;o>>=1) s+=__shfl_xor(s,o);
  float m=s/D; float q=0;
  for(int i=0;i<7;i++){ float d=v[i]-m; q+=d*d; }
  for(int o=32;o;o>>=1) q+=__shfl_xor(q,o);
  float r=rsqrtf(q/D+1e-5f);
  for(int i=0;i<7;i++) y[row*D+t+64*i]=f2bf((v[i]-m)*r);
}
__global__ void k_gemm(const u16* A,const u16* B,u16* C,const u16* res,int M,int N,int K){
  __shared__ float As[16][17], Bs[16][17];
  int tx=threadIdx.x&15, ty=threadIdx.x>>4;
  int row=blockIdx.y*16+ty, col=blockIdx.x*16+tx;
  float acc=0;
  for(int k0=0;k0<K;k0+=16){
    As[ty][tx]=bf2f(A[row*K+k0+tx]);
    Bs[ty][tx]=bf2f(B[(k0+ty)*N+col]);
    __syncthreads();
    for(int k=0;k<16;k++) acc+=As[ty][k]*Bs[k][tx];
    __syncthreads();
  }
  float o=bf2f(f2bf(acc));
  if(res) o+=bf2f(res[row*N+col]);
  C[row*N+col]=f2bf(o);
}
__global__ void k_attn(const u16* qkv,u16* o){
  int q=blockIdx.x, h=blockIdx.y, t=threadIdx.x;
  __shared__ float p[256]; __shared__ float red[4];
  int k=q-255+t; float s=-1e30f;
  if(k>=0){ float a=0; for(int d=0;d<DH;d++) a+=bf2f(qkv[q*1344+h*DH+d])*bf2f(qkv[k*1344+448+h*DH+d]); s=a*0.18898223650461362f; }
  float m=s; for(int of=32;of;of>>=1) m=fmaxf(m,__shfl_xor(m,of));
  if((t&63)==0) red[t>>6]=m; __syncthreads();
  m=fmaxf(fmaxf(red[0],red[1]),fmaxf(red[2],red[3])); __syncthreads();
  float e=(k>=0)?__expf(s-m):0.f; float z=e;
  for(int of=32;of;of>>=1) z+=__shfl_xor(z,of);
  if((t&63)==0) red[t>>6]=z; __syncthreads();
  z=red[0]+red[1]+red[2]+red[3];
  p[t]=bf2f(f2bf(e/z)); __syncthreads();
  if(t<DH){ float a=0; for(int j=0;j<256;j++){ int kk=q-255+j; if(kk>=0) a+=p[j]*bf2f(qkv[kk*1344+896+h*DH+t]); } o[q*D+h*DH+t]=f2bf(a); }
}
__global__ void k_glu(const u16* g,const u16* u,u16* h,int n){
  int i=blockIdx.x*256+threadIdx.x; if(i>=n) return;
  float x=bf2f(g[i]); float t=tanhf(0.7978845608028654f*(x+0.044715f*x*x*x));
  float ge=bf2f(f2bf(0.5f*x*(1.f+t)));
  h[i]=f2bf(ge*bf2f(u[i]));
}
extern "C" void kernel_launch(void* const* d_in, const int* in_sizes, int n_in, void* d_out, int out_size, void* d_ws, size_t ws_size, hipStream_t stream){
  const u16* x=(const u16*)d_in[0]; const u16* wqkv=(const u16*)d_in[1]; const u16* wo=(const u16*)d_in[2];
  const u16* wg=(const u16*)d_in[4]; const u16* wu=(const u16*)d_in[5]; const u16* wd=(const u16*)d_in[6];
  u16* ws=(u16*)d_ws;
  u16* xn=ws; u16* qkv=xn+S*D; u16* o=qkv+S*1344; u16* x1=o+S*D; u16* g=x1+S*D; u16* u=g+S*F; u16* h=u+S*F; u16* x2=h+S*F;
  const u16* cur=x;
  for(int l=0;l<2;l++){
    u16* outp = l==0? x2 : (u16*)d_out;
    k_ln<<<S,64,0,stream>>>(cur,xn);
    k_gemm<<<dim3(1344/16,S/16),256,0,stream>>>(xn,wqkv,qkv,nullptr,S,1344,D);
    k_attn<<<dim3(S,H),256,0,stream>>>(qkv,o);
    k_gemm<<<dim3(D/16,S/16),256,0,stream>>>(o,wo,x1,cur,S,D,D);
    k_ln<<<S,64,0,stream>>>(x1,xn);
    k_gemm<<<dim3(F/16,S/16),256,0,stream>>>(xn,wg,g,nullptr,S,F,D);
    k_gemm<<<dim3(F/16,S/16),256,0,stream>>>(xn,wu,u,nullptr,S,F,D);
    k_glu<<<S*F/256,256,0,stream>>>(g,u,h,S*F);
    k_gemm<<<dim3(D/16,S/16),256,0,stream>>>(h,wd,outp,x1,S,D,F);
    cur=x2;
  }
}
```

```cpp
#include <hip/hip_runtime.h>
#include <stdint.h>
#include <type_traits>

namespace {

typedef unsigned short u16;
typedef short bf16x8 __attribute__((ext_vector_type(8)));
typedef short s16x4 __attribute__((ext_vector_type(4)));
typedef __bf16 bf16x2 __attribute__((ext_vector_type(2)));
typedef float f32x2 __attribute__((ext_vector_type(2)));
typedef float f32x4 __attribute__((ext_vector_type(4)));
typedef float f32x16 __attribute__((ext_vector_type(16)));
typedef unsigned v4u __attribute__((ext_vector_type(4)));
typedef unsigned v2u __attribute__((ext_vector_type(2)));
#define LAS __attribute__((address_space(3)))
#define GAS __attribute__((address_space(1)))
#define DI __device__ __forceinline__

constexpr unsigned KB = 1024;
constexpr unsigned OFF_W1T = 0;
constexpr unsigned OFF_W4T = OFF_W1T + 48 * 29 * KB;
constexpr unsigned OFF_W5T = OFF_W4T + 112 * 29 * KB;
constexpr unsigned OFF_W3T = OFF_W5T + 16 * 112 * KB;
constexpr unsigned OFF_X0 = OFF_W3T + 16 * 32 * KB;
constexpr unsigned OFF_L = OFF_X0 + 64 * 28 * KB;
constexpr unsigned L_KV = 0;
constexpr unsigned L_OF = L_KV + 4096 * KB;
constexpr unsigned L_X1 = L_OF + 2048 * KB;
constexpr unsigned L_S1 = L_X1 + 1792 * KB;
constexpr unsigned L_HF = L_S1 + 128 * KB;
constexpr unsigned L_X2 = L_HF + 7168 * KB;
constexpr unsigned L_S2 = L_X2 + 1792 * KB;
constexpr unsigned L_BYTES = L_S2 + 128 * KB;
constexpr unsigned OFF_DBG = OFF_L + 2 * L_BYTES;
constexpr unsigned WS_BYTES = OFF_DBG + 1024 * KB;
constexpr unsigned SENT = 0xFFFFFFFFu;
constexpr int AUX_SC1 = 16;
constexpr int TRY_CAP = 20000;

constexpr unsigned R1 = 0;
constexpr unsigned R2 = 87 * KB;
constexpr unsigned R3 = R2 + 58 * KB;
constexpr unsigned LDS_BYTES = R3 + 1 * KB;

DI unsigned pk(float a, float b) {
  f32x2 v = {a, b};
  bf16x2 r = __builtin_convertvector(v, bf16x2);
  return __builtin_bit_cast(unsigned, r);
}
DI float bflo(unsigned d) { return __uint_as_float(d << 16); }
DI float bfhi(unsigned d) { return __uint_as_float(d & 0xffff0000u); }
DI float rbf(float a) { return bflo(pk(a, a)); }
DI f32x16 mfma(v4u a, v4u b, f32x16 c) {
  return __builtin_amdgcn_mfma_f32_32x32x16_bf16(__builtin_bit_cast(bf16x8, a), __builtin_bit_cast(bf16x8, b), c, 0, 0, 0);
}
DI f32x16 zero16() {
  f32x16 z;
#pragma unroll
  for (int i = 0; i < 16; ++i) z[i] = 0.f;
  return z;
}
DI unsigned umax3(unsigned a, unsigned b, unsigned c) { return max(max(a, b), c); }
DI unsigned mx4(unsigned m, v4u v) { return umax3(umax3(m, v.x, v.y), v.z, v.w); }
DI unsigned clampS(unsigned d) { return min(d, 0xFFFFFFFEu); }
DI v4u clampS4(v4u v) { return v4u{clampS(v.x), clampS(v.y), clampS(v.z), clampS(v.w)}; }
DI float add32(float v) {
  unsigned u = __float_as_uint(v);
  auto r = __builtin_amdgcn_permlane32_swap(u, u, false, false);
  return __uint_as_float(r[0]) + __uint_as_float(r[1]);
}
DI float add16(float v) {
  unsigned u = __float_as_uint(v);
  auto r = __builtin_amdgcn_permlane16_swap(u, u, false, false);
  return __uint_as_float(r[0]) + __uint_as_float(r[1]);
}
DI float upper32(float v) {
  unsigned u = __float_as_uint(v);
  auto r = __builtin_amdgcn_permlane32_swap(u, u, false, false);
  return __uint_as_float(r[1]);
}
DI float lower32(float v) {
  unsigned u = __float_as_uint(v);
  auto r = __builtin_amdgcn_permlane32_swap(u, u, false, false);
  return __uint_as_float(r[0]);
}
DI v4u pack8(const f32x16& a, int s, float r) {
  return v4u{pk(a[8 * s] * r, a[8 * s + 1] * r), pk(a[8 * s + 2] * r, a[8 * s + 3] * r), pk(a[8 * s + 4] * r, a[8 * s + 5] * r),
             pk(a[8 * s + 6] * r, a[8 * s + 7] * r)};
}
typedef __amdgpu_buffer_rsrc_t rsrc_t;
DI v4u ldc(rsrc_t rs, unsigned voff, unsigned soff) { return __builtin_amdgcn_raw_buffer_load_b128(rs, voff, soff, AUX_SC1); }
DI v2u ldc64(rsrc_t rs, unsigned voff, unsigned soff) { return __builtin_amdgcn_raw_buffer_load_b64(rs, voff, soff, AUX_SC1); }
DI v4u ldp(rsrc_t rs, unsigned voff, unsigned soff) { return __builtin_amdgcn_raw_buffer_load_b128(rs, voff, soff, 0); }
DI void stc(rsrc_t rs, v4u v, unsigned voff, unsigned soff) { __builtin_amdgcn_raw_buffer_store_b128(v, rs, voff + soff, 0, AUX_SC1); }
DI void stc64(rsrc_t rs, v2u v, unsigned voff, unsigned soff) { __builtin_amdgcn_raw_buffer_store_b64(v, rs, voff, soff, AUX_SC1); }
DI v4u lds_rd(LAS unsigned char* p) { return *(LAS v4u*)p; }
DI unsigned opq(unsigned v) { asm volatile("" : "+v"(v)); return v; }
DI LAS unsigned char* opq(LAS unsigned char* p) { asm volatile("" : "+v"(p)); return p; }
DI void lds_wr(LAS unsigned char* p, v4u v) { *(LAS v4u*)p = v; }
#define VMWAIT(n) asm volatile("s_waitcnt vmcnt(" #n ")" ::: "memory")
#define LGKM0() asm volatile("s_waitcnt lgkmcnt(0)" ::: "memory")
DI void bar() {
  LGKM0();
  __builtin_amdgcn_s_barrier();
  asm volatile("" ::: "memory");
}

DI void dma4(const unsigned char* g, unsigned voff, unsigned la) {
  unsigned keep;
  asm volatile(
      "s_nop 4\n\ts_mov_b32 %0, m0\n\ts_mov_b32 m0, %3\n\ts_nop 0\n\t"
      "global_load_lds_dwordx4 %1, %2\n\t"
      "global_load_lds_dwordx4 %1, %2 offset:1024\n\t"
      "global_load_lds_dwordx4 %1, %2 offset:2048\n\t"
      "global_load_lds_dwordx4 %1, %2 offset:3072\n\t"
      "s_mov_b32 m0, %0"
      : "=&s"(keep)
      : "v"(voff), "s"(g), "s"(la)
      : "memory");
}
DI void dma1(const unsigned char* g, unsigned voff, unsigned la) {
  unsigned keep;
  asm volatile(
      "s_nop 4\n\ts_mov_b32 %0, m0\n\ts_mov_b32 m0, %3\n\ts_nop 0\n\t"
      "global_load_lds_dwordx4 %1, %2\n\t"
      "s_mov_b32 m0, %0"
      : "=&s"(keep)
      : "v"(voff), "s"(g), "s"(la)
      : "memory");
}
DI void dma_copy(const unsigned char* ws, unsigned goff, unsigned la, int n, int w, unsigned lane16) {
  for (int c = 4 * w; c < n; c += 16) {
    const unsigned char* g = ws + goff + (unsigned)c * KB;
    if (n - c >= 4) dma4(g, lane16, la + (unsigned)c * KB);
    else
      for (int i = 0; i < n - c; ++i) dma1(g + (unsigned)i * KB, lane16, la + (unsigned)(c + i) * KB);
  }
}

#define STAMP(i)

__global__ __launch_bounds__(256, 2) void k_prep(const u16* __restrict__ x, const u16* __restrict__ wqkv, const u16* __restrict__ wo,
                                              const u16* __restrict__ wg, const u16* __restrict__ wu, const u16* __restrict__ wd,
                                              unsigned char* __restrict__ ws) {
  __shared__ __attribute__((aligned(16))) unsigned char smem[452 * 64 + 512];
  LAS unsigned char* img = (LAS unsigned char*)smem;
  const int b = blockIdx.x, tid = threadIdx.x, lane = tid & 63, w = __builtin_amdgcn_readfirstlane(tid >> 6);
  const int h = lane >> 5;

  const int row = 8 * b + 2 * w + h, li = lane & 31;
  v4u xa = {0, 0, 0, 0}, xb = {0, 0, 0, 0};
  if (li < 28) {
    const v4u* p = (const v4u*)(x + row * 448 + li * 16);
    xa = p[0];
    xb = p[1];
  }

  const u16 *pA = wo, *pB = wo;
  int stride = 448, nvalid = 0, kind = 3;
  unsigned dst = 0;
  if (b < 48) {
    const int head = b / 3, part = b - 3 * head;
    pA = wqkv + (part == 0 ? 448 : (part == 1 ? 896 : 0)) + 28 * head;
    pB = pA + 16;
    stride = 1344; nvalid = 7; kind = 0; dst = OFF_W1T + (unsigned)b * 29 * KB;
  } else if (b < 160) {
    const int i = b - 48;
    pA = wg + 16 * i; pB = wu + 16 * i;
    stride = 1792; nvalid = 8; kind = 0; dst = OFF_W4T + (unsigned)i * 29 * KB;
  } else if (b < 224) {
    const int i = b - 160, cn = i >> 2, q = i & 3;
    pA = wd + q * 448 * 448 + 56 * (cn >> 1) + 32 * (cn & 1);
    pB = pA + 16;
    nvalid = (cn & 1) ? 6 : 8; kind = 1; dst = OFF_W5T + (unsigned)(cn * 112 + q * 28) * KB;
  } else if (b < 240) {
    const int cn = b - 224;
    pA = wo + 56 * (cn >> 1) + 32 * (cn & 1);
    pB = pA + 16;
    nvalid = (cn & 1) ? 6 : 8; kind = 2; dst = OFF_W3T + (unsigned)cn * 32 * KB;
  }
  if (kind < 3) {
    const int u = lane & 7;
    const u16* src = (u < 4 ? pA : pB) + (u < nvalid ? 4 * (u & 3) : 0) + (112 * w + (lane >> 3)) * stride;
    v2u t[14];
#pragma unroll
    for (int i = 0; i < 14; ++i) t[i] = *(const v2u*)(src + 8 * i * stride);
    const unsigned keep = (u < nvalid) ? 0xFFFFFFFFu : 0u;
#pragma unroll
    for (int i = 0; i < 14; ++i) *(LAS v2u*)(img + (112 * w + 8 * i) * 64 + lane * 8) = v2u{t[i].x & keep, t[i].y & keep};
  }

  {
    const v4u s4 = {SENT, SENT, SENT, SENT};
    v4u* p = (v4u*)(ws + OFF_L);
    constexpr unsigned N16 = 2 * L_BYTES / 16;
    for (unsigned i = b * 256 + tid; i < N16; i += 65536) p[i] = s4;
  }

  {
    float v[16];
    const unsigned d[8] = {xa.x, xa.y, xa.z, xa.w, xb.x, xb.y, xb.z, xb.w};
#pragma unroll
    for (int i = 0; i < 8; ++i) { v[2 * i] = bflo(d[i]); v[2 * i + 1] = bfhi(d[i]); }
    float s = 0.f;
#pragma unroll
    for (int i = 0; i < 16; ++i) s += v[i];
#pragma unroll
    for (int o = 16; o; o >>= 1) s += __shfl_xor(s, o);
    const float mean = s / 448.f;
    float q = 0.f;
#pragma unroll
    for (int i = 0; i < 16; ++i) { const float e = v[i] - mean; q += e * e; }
    if (li >= 28) q = 0.f;
#pragma unroll
    for (int o = 16; o; o >>= 1) q += __shfl_xor(q, o);
    const float mb = rbf(mean), den = rbf(sqrtf(rbf(rbf(q / 448.f) + 1e-5f)));
    unsigned o8[8];
#pragma unroll
    for (int i = 0; i < 8; ++i) o8[i] = pk(rbf(v[2 * i] - mb) / den, rbf(v[2 * i + 1] - mb) / den);
    if (li < 28) {
      unsigned char* p = ws + OFF_X0 + (unsigned)((row >> 5) * 28 + li) * KB + (row & 31) * 16;
      *(v4u*)p = v4u{o8[0], o8[1], o8[2], o8[3]};
      *(v4u*)(p + 512) = v4u{o8[4], o8[5], o8[6], o8[7]};
    }
  }

  if (kind < 3) {
    LGKM0();
    const int blk = (lane >> 4) & 1, q = (lane & 15) >> 2, p = lane & 3;
    const int dlo = (kind == 0) ? 8 * h : 4 * h, dhi = (kind == 0) ? 4 : 8;
    LAS unsigned char* base = img + (112 * w + dlo + q) * 64 + blk * 32 + p * 8;
    const int nfr = (kind == 2) ? 8 : 7;
    f32x16 cs = zero16();
    const v4u ones = {0x3F803F80u, 0x3F803F80u, 0x3F803F80u, 0x3F803F80u};
    for (int f = 0; f < nfr; ++f) {
      const int rb = (kind == 2) ? 28 * (f >> 1) + 16 * (f & 1) : 16 * f;
      s16x4 lo = __builtin_amdgcn_ds_read_tr16_b64_v4i16((LAS s16x4*)(base + rb * 64));
      s16x4 hi = __builtin_amdgcn_ds_read_tr16_b64_v4i16((LAS s16x4*)(base + (rb + dhi) * 64));
      v2u l2 = __builtin_bit_cast(v2u, lo), h2 = __builtin_bit_cast(v2u, hi);
      if (kind == 2 && (f & 1) && h) h2 = v2u{0, 0};
      const v4u fr = {l2.x, l2.y, h2.x, h2.y};
      *(v4u*)(ws + dst + (unsigned)(nfr * w + f) * KB + lane * 16) = fr;
      cs = mfma(ones, fr, cs);
    }
    if (kind == 0) {
      LAS float* red = (LAS float*)(img + 452 * 64);
      if (lane < 32) red[w * 32 + lane] = cs[0];
      __syncthreads();
      if (w == 0) {
        const int n = lane & 31;
        const float c = (red[n] + red[32 + n]) + (red[64 + n] + red[96 + n]);
        const float c0 = rbf(c), c1 = rbf(c - c0), c2 = rbf(c - c0 - c1);
        v4u fr = {pk(c0, c1), pk(c2, 0.f), 0, 0};
        if (h) fr = v4u{0, 0, 0, 0};
        *(v4u*)(ws + dst + 28 * KB + lane * 16) = fr;
      }
    }
  }
}

#define ACLOB "a0","a1","a2","a3","a4","a5","a6","a7","a8","a9","a10","a11","a12","a13","a14","a15","a16","a17","a18","a19","a20","a21","a22","a23","a24","a25","a26","a27","a28","a29","a30","a31","a32","a33","a34","a35","a36","a37","a38","a39","a40","a41","a42","a43","a44","a45","a46","a47","a48","a49","a50","a51","a52","a53","a54","a55","a56","a57","a58","a59","a60","a61","a62","a63","a64","a65","a66","a67","a68","a69","a70","a71","a72","a73","a74","a75","a76","a77","a78","a79","a80","a81","a82","a83","a84","a85","a86","a87","a88","a89","a90","a91","a92","a93","a94","a95","a96","a97","a98","a99","a100","a101","a102","a103","a104","a105","a106","a107","a108","a109","a110","a111","a112","a113","a114","a115","a116","a117","a118","a119","a120","a121","a122","a123","a124","a125","a126","a127","a128","a129","a130","a131","a132","a133","a134","a135","a136","a137","a138","a139","a140","a141","a142","a143","a144","a145","a146","a147","a148","a149","a150","a151","a152","a153","a154","a155","a156","a157","a158","a159","a160","a161","a162","a163","a164","a165","a166","a167","a168","a169","a170","a171","a172","a173","a174","a175","a176","a177","a178","a179","a180","a181","a182","a183","a184","a185","a186","a187","a188","a189","a190","a191","a192","a193","a194","a195","a196","a197","a198","a199","a200","a201","a202","a203","a204","a205","a206","a207","a208","a209","a210","a211","a212","a213","a214","a215","a216","a217","a218","a219","a220","a221","a222","a223","a224","a225","a226","a227","a228","a229","a230","a231","a232","a233","a234","a235","a236","a237","a238","a239","a240","a241","a242","a243","a244","a245","a246","a247","a248","a249","a250","a251","a252","a253","a254","a255"
#define GAP() __builtin_amdgcn_sched_barrier(0)
#define MFMA_OP "v_mfma_f32_32x32x16_bf16 "
template <int N, int I = 0, class F> DI void sfor(F&& f) {
  if constexpr (I < N) {
    f(std::integral_constant<int, I>{});
    sfor<N, I + 1>(f);
  }
}
DI void mf(f32x16& c, v4u a, v4u b) { asm volatile(MFMA_OP "%0, %1, %2, %0" : "+v"(c) : "v"(a), "v"(b) : ACLOB); }
DI void mf0(f32x16& c, v4u a, v4u b) { asm volatile(MFMA_OP "%0, %1, %2, 0" : "=&v"(c) : "v"(a), "v"(b) : ACLOB); }
DI void mfn(f32x16& c, v4u a, v4u b) { asm volatile("s_nop 1\n\t" MFMA_OP "%0, %1, %2, %0" : "+v"(c) : "v"(a), "v"(b) : ACLOB); }
DI void mfn0(f32x16& c, v4u a, v4u b) { asm volatile("s_nop 1\n\t" MFMA_OP "%0, %1, %2, 0" : "=&v"(c) : "v"(a), "v"(b) : ACLOB); }
template <int A> DI void mfa(f32x16& c, v4u b) {
  asm volatile(MFMA_OP "%0, a[%c2:%c3], %1, %0" : "+v"(c) : "v"(b), "i"(A), "i"(A + 3) : ACLOB);
}
template <int A> DI void mfa0(f32x16& c, v4u b) {
  asm volatile(MFMA_OP "%0, a[%c2:%c3], %1, 0" : "=&v"(c) : "v"(b), "i"(A), "i"(A + 3) : ACLOB);
}
DI void pin(f32x16& c) { asm volatile("" : "+v"(c)); }
DI void mfence(f32x16& c) { asm volatile("s_nop 7\n\ts_nop 3" : "+v"(c)); }
template <int A> DI void lda4(const unsigned char* g, unsigned voff) {
  asm volatile(
      "s_nop 4\n\t"
      "global_load_dwordx4 a[%c2:%c3], %0, %1\n\t"
      "global_load_dwordx4 a[%c4:%c5], %0, %1 offset:1024\n\t"
      "global_load_dwordx4 a[%c6:%c7], %0, %1 offset:2048\n\t"
      "global_load_dwordx4 a[%c8:%c9], %0, %1 offset:3072"
      :
      : "v"(voff), "s"(g), "i"(A), "i"(A + 3), "i"(A + 4), "i"(A + 7), "i"(A + 8), "i"(A + 11), "i"(A + 12), "i"(A + 15)
      : "memory", ACLOB);
}
template <int A> DI void lda2(const unsigned char* g, unsigned voff) {
  asm volatile(
      "s_nop 4\n\t"
      "global_load_dwordx4 a[%c2:%c3], %0, %1\n\t"
      "global_load_dwordx4 a[%c4:%c5], %0, %1 offset:1024"
      :
      : "v"(voff), "s"(g), "i"(A), "i"(A + 3), "i"(A + 4), "i"(A + 7)
      : "memory", ACLOB);
}
DI float max3f(float a, float b, float c) { float r; asm("v_max3_f32 %0, %1, %2, %3" : "=v"(r) : "v"(a), "v"(b), "v"(c)); return r; }
DI float fmul_s(float a, float b) { float r; asm("v_mul_f32_e32 %0, %1, %2" : "=v"(r) : "v"(a), "v"(b)); return r; }
DI float ffma_s(float a, float b, float c) { float r; asm("v_fma_f32 %0, %1, %2, %3" : "=v"(r) : "v"(a), "v"(b), "v"(c)); return r; }

DI void wait_valid(rsrc_t rs, unsigned voff, unsigned soff, int n, unsigned stride) {
#pragma clang loop unroll(disable)
  for (int tries = 0; tries < TRY_CAP; ++tries) {
    unsigned m = 0;
#pragma unroll 4
    for (int i = 0; i < n; ++i) m = mx4(m, ldc(rs, voff, soff + (unsigned)i * stride));
    if (!__any(m == SENT)) break;
    __builtin_amdgcn_s_sleep(16);
  }
}

struct LnStat { float r, rinv, q; v4u corr; };

DI LnStat ln_stat(const v2u (&st)[4], bool raw, int h) {
  float s = (__uint_as_float(st[0].x) + __uint_as_float(st[1].x)) + (__uint_as_float(st[2].x) + __uint_as_float(st[3].x));
  float q = (__uint_as_float(st[0].y) + __uint_as_float(st[1].y)) + (__uint_as_float(st[2].y) + __uint_as_float(st[3].y));
  s = add32(s);
  q = add32(q);
  const float mean = s * (1.f / 448.f);
  const float var = fmaxf(q * (1.f / 448.f) - mean * mean, 0.f);
  const float den = rbf(__builtin_amdgcn_sqrtf(rbf(rbf(var) + 1e-5f)));
  LnStat o;
  o.q = raw ? q : 0.f;
  o.rinv = raw ? den : 1.f;
  o.r = raw ? __builtin_amdgcn_rcpf(den) : 1.f;
  const unsigned nm = pk(-mean, -mean);
  const bool on = raw && !h;
  o.corr = v4u{on ? nm : 0u, on ? (nm & 0xffffu) : 0u, 0u, 0u};
  return o;
}

template <int I> DI void dma_piece(const unsigned char* g, unsigned voff, unsigned la) {
  asm volatile("s_mov_b32 m0, %2\n\ts_nop 0\n\tglobal_load_lds_dwordx4 %0, %1 offset:%c3" ::"v"(voff), "s"(g), "s"(la), "i"(I * 1024) : "memory");
}
template <int N, int PER, int I> DI void dma_share(const unsigned char* ws, unsigned goff, unsigned la, int w, unsigned lane16) {
  const unsigned first = (unsigned)min(w * PER, N - PER) * KB + (I & ~3) * KB;
  dma_piece<I & 3>(ws + goff + first, lane16, la + first);
}
template <int A, int OFF> DI void lda1(const unsigned char* g, unsigned voff) {
  asm volatile("global_load_dwordx4 a[%c2:%c3], %0, %1 offset:%c4" ::"v"(voff), "s"(g), "i"(A), "i"(A + 3), "i"(OFF) : "memory", ACLOB);
}
DI unsigned st_max(const v2u (&st)[4]) {
  return umax3(umax3(umax3(st[0].x, st[0].y, st[1].x), st[1].y, st[2].x), umax3(st[2].y, st[3].x, st[3].y), 0u);
}
constexpr int XD = 12;

template <class F>
DI void x_enter(rsrc_t rs, unsigned xo, unsigned so, bool raw, int lane, v4u (&xb)[29], v2u (&st)[4], F&& once) {
#pragma clang loop unroll(disable)
  for (int t2 = 0;; ++t2) {
    asm volatile("" : "+s"(t2)::"memory");
    asm volatile("" : "+v"(lane));
    const unsigned sv = (unsigned)((lane >> 5) * 1024 + (lane & 31) * 8), l16 = lane * 16;
#pragma unroll
    for (int i = 0; i < 4; ++i) st[i] = ldc64(rs, sv + i * 256, so);
#pragma unroll
    for (int k = 0; k < XD; ++k) xb[k] = ldc(rs, l16 + k * KB, xo);
    GAP();
    if (t2 == 0) once();
    if (t2 || !raw || !__any(st_max(st) == SENT)) break;
#pragma clang loop unroll(disable)
    for (int tries = 0; tries < TRY_CAP; ++tries) {
      __builtin_amdgcn_s_sleep(4);
      v2u p[4];
#pragma unroll
      for (int i = 0; i < 4; ++i) p[i] = ldc64(rs, sv + i * 256, so);
      if (!__any(st_max(p) == SENT)) break;
    }
  }
}

template <bool SWAP1>
DI void gemm3(LAS unsigned char* W, rsrc_t rs, unsigned xo, unsigned lane16, v4u (&xb)[29], const v2u (&st)[4], bool raw, int h, f32x16& a0,
              f32x16& a1, f32x16& a2, LnStat& ls) {
  v4u ab[3][2];
  W = opq(W);
  lane16 = opq(lane16);
  LAS unsigned char* W2 = opq(W + 58 * KB);
#pragma unroll
  for (int t = 0; t < 2; ++t) {
    ab[t][0] = lds_rd(W + (t * 29 + 0) * KB);
    ab[t][1] = lds_rd(W + (t * 29 + 1) * KB);
  }
  ab[2][0] = lds_rd(W2);
  ab[2][1] = lds_rd(W2 + KB);
  GAP();
  sfor<28>([&](auto K) {
    constexpr int k = K;
    if constexpr (k == 0) mf0(a0, ab[0][0], xb[0]); else mf(a0, ab[0][k & 1], xb[k]);
    if constexpr (k + 2 <= 28) ab[0][k & 1] = lds_rd(W + (0 * 29 + k + 2) * KB);
    if constexpr (k + XD < 28) xb[k + XD] = ldc(rs, lane16 + (k + XD) * KB, xo);
    if constexpr (SWAP1) {
      if constexpr (k == 0) mf0(a1, xb[0], ab[1][0]); else mf(a1, xb[k], ab[1][k & 1]);
    } else {
      if constexpr (k == 0) mf0(a1, ab[1][0], xb[0]); else mf(a1, ab[1][k & 1], xb[k]);
    }
    if constexpr (k + 2 <= 28) ab[1][k & 1] = lds_rd(W + (1 * 29 + k + 2) * KB);
    if constexpr (k == 0) mf0(a2, ab[2][0], xb[0]); else mf(a2, ab[2][k & 1], xb[k]);
    if constexpr (k + 2 <= 28) ab[2][k & 1] = lds_rd(W2 + (k + 2) * KB);
    if constexpr (k == 20) {
      ls = ln_stat(st, raw, h);
      xb[28] = ls.corr;
    }
    GAP();
  });
  mfn(a0, ab[0][0], xb[28]);
  if constexpr (SWAP1) mf(a1, xb[28], ab[1][0]); else mf(a1, ab[1][0], xb[28]);
  mf(a2, ab[2][0], xb[28]);
  asm volatile("s_nop 7\n\ts_nop 3" : "+v"(a0), "+v"(a1), "+v"(a2));
}

DI void unpack_res(const v4u (&o)[2], f32x4 (&res)[2][2]) {
#pragma unroll
  for (int rd = 0; rd < 2; ++rd) {
    res[rd][0] = f32x4{bflo(o[rd].x), bfhi(o[rd].x), bflo(o[rd].y), bfhi(o[rd].y)};
    res[rd][1] = f32x4{bflo(o[rd].z), bfhi(o[rd].z), bflo(o[rd].w), bfhi(o[rd].w)};
  }
}
DI void reduce_tile(LAS unsigned char* L, f32x16 (&acc)[2][2], int w, int lane, v4u (&o)[2]) {
  f32x4 res[2][2];
  LAS unsigned char* wp = opq(L + R2 + (unsigned)(w * 14) * KB + lane * 16);
#pragma unroll
  for (int tt = 0; tt < 2; ++tt)
#pragma unroll
    for (int e = 0; e < 7; ++e) {
      const f32x16& a = acc[tt][e >> 2];
      const int i = e & 3;
      f32x4 v = {a[4 * i], a[4 * i + 1], a[4 * i + 2], a[4 * i + 3]};
      *(LAS f32x4*)(wp + (tt * 7 + e) * KB) = v;
    }
  unpack_res(o, res);
  bar();
  const int g = lane >> 4, tk = (w & 1) * 16 + (lane & 15), tt = w >> 1;
#pragma unroll
  for (int rd = 0; rd < 2; ++rd) {
    const int e = min(g + 4 * rd, 6);
    LAS unsigned char* p = opq(L + R2 + (unsigned)(tt * 7 + e) * KB + tk * 16);
    f32x4 lo = res[rd][0], hi = res[rd][1];
    f32x4 l4[4], h4[4];
#pragma unroll
    for (int kq = 0; kq < 4; ++kq) {
      l4[kq] = *(LAS f32x4*)(p + kq * 14 * KB);
      h4[kq] = *(LAS f32x4*)(p + kq * 14 * KB + 512);
    }
    lo += (l4[0] + l4[1]) + (l4[2] + l4[3]);
    hi += (h4[0] + h4[1]) + (h4[2] + h4[3]);
    o[rd] = clampS4(v4u{pk(lo[0], lo[1]), pk(lo[2], lo[3]), pk(hi[0], hi[1]), pk(hi[2], hi[3])});
  }
}
DI void publish_x(rsrc_t rs, unsigned xoff, unsigned soff, int T0, int c, int w, int lane, const v4u (&o)[2]) {
  f32x4 res[2][2];
  const int g = lane >> 4, tk = (w & 1) * 16 + (lane & 15), T = T0 + (w >> 1);
  const unsigned xv = (unsigned)((7 * c + g) * 512 + tk * 16);
  stc(rs, o[0], xv, xoff + (unsigned)T * 28 * KB);
  if (g < 3) stc(rs, o[1], xv + 2048, xoff + (unsigned)T * 28 * KB);
  unpack_res(o, res);
  f32x4 s4 = res[0][0] + res[0][1], q4 = res[0][0] * res[0][0] + res[0][1] * res[0][1];
  const f32x4 s5 = res[1][0] + res[1][1], q5 = res[1][0] * res[1][0] + res[1][1] * res[1][1];
  float s = (s4[0] + s4[1]) + (s4[2] + s4[3]), q = (q4[0] + q4[1]) + (q4[2] + q4[3]);
  const float s1 = (s5[0] + s5[1]) + (s5[2] + s5[3]), q1 = (q5[0] + q5[1]) + (q5[2] + q5[3]);
  if (g < 3) { s += s1; q += q1; }
  s = add32(add16(s));
  q = add32(add16(q));
  if (lane < 16) stc64(rs, v2u{clampS(__float_as_uint(s)), clampS(__float_as_uint(q))}, (unsigned)(tk * 8), soff + (unsigned)(T * 8 + c) * 256);
}

struct GeluSt { float a0, a1, t0, t1; };
template <int STG> DI void gelu_stage(GeluSt& s, float g0, float u0, float g1, float u1, float r, float rinv, float k1, float k2, unsigned& out) {
  if constexpr (STG == 0) {
    asm volatile(
        "v_mul_f32 %0, %4, %6\n\tv_mul_f32 %1, %5, %6\n\tv_mul_f32 %2, %0, %0\n\tv_mul_f32 %3, %1, %1\n\tv_fma_f32 %2, %2, %8, %7"
        : "=&v"(s.a0), "=&v"(s.a1), "=&v"(s.t0), "=&v"(s.t1)
        : "v"(g0), "v"(g1), "v"(r), "v"(k1), "v"(k2));
  } else if constexpr (STG == 1) {
    asm volatile("v_fma_f32 %1, %1, %5, %4\n\tv_mul_f32 %0, %2, %0\n\tv_mul_f32 %1, %3, %1\n\tv_exp_f32 %0, %0"
                 : "+v"(s.t0), "+v"(s.t1)
                 : "v"(s.a0), "v"(s.a1), "v"(k1), "v"(k2));
  } else if constexpr (STG == 2) {
    asm volatile("v_exp_f32 %3, %3\n\tv_mul_f32 %0, %0, %4\n\tv_mul_f32 %1, %1, %5\n\tv_fma_f32 %2, %2, %6, %6"
                 : "+v"(s.a0), "+v"(s.a1), "+v"(s.t0), "+v"(s.t1)
                 : "v"(u0), "v"(u1), "v"(rinv));
  } else if constexpr (STG == 3) {
    asm volatile("v_fma_f32 %1, %1, %2, %2\n\tv_rcp_f32 %0, %0\n\tv_rcp_f32 %1, %1" : "+v"(s.t0), "+v"(s.t1) : "v"(rinv));
  } else {
    asm volatile("v_mul_f32 %1, %1, %3\n\tv_mul_f32 %2, %2, %4\n\tv_cvt_pk_bf16_f32 %0, %1, %2\n\tv_min_u32 %0, -2, %0"
                 : "=&v"(out), "+v"(s.a0), "+v"(s.a1)
                 : "v"(s.t0), "v"(s.t1));
  }
}

__global__ __launch_bounds__(256, 1) void k_main(const u16* __restrict__ x, u16* __restrict__ out, unsigned char* __restrict__ ws) {
  __shared__ __attribute__((aligned(16))) unsigned char smem[LDS_BYTES];
  LAS unsigned char* L = (LAS unsigned char*)smem;
  const unsigned lbase = (unsigned)(size_t)L;
  int b = blockIdx.x, lane = threadIdx.x & 63, w = __builtin_amdgcn_readfirstlane(threadIdx.x >> 6);
  int h, g, j, c, T0, T;
  unsigned lane16;
#define FRESH_IDS()                                                  \
  do {                                                               \
    asm volatile("" : "+v"(lane));                                   \
    asm volatile("" : "+s"(w));                                      \
    asm volatile("" : "+s"(b));                                      \
    h = lane >> 5;                                                   \
    g = (b & 7) * 2 + (b >> 7);                                      \
    j = (b >> 3) & 15;                                               \
    c = j >> 1;                                                      \
    T0 = 4 * g + 2 * (j & 1);                                        \
    T = 4 * g + w;                                                   \
    lane16 = lane * 16;                                              \
  } while (0)
  FRESH_IDS();
  const rsrc_t rs = __builtin_amdgcn_make_buffer_rsrc((void*)ws, 0, (int)WS_BYTES, 0x00020000);
  STAMP(0);

  sfor<22>([&](auto I) { dma_share<87, 22, I>(ws, OFF_W1T + (unsigned)j * 87 * KB, lbase + R1, w, lane16); });

  v4u res[2];
  {
    const int tok = 32 * (T0 + (w >> 1)) + 16 * (w & 1) + (lane & 15), gq = lane >> 4;
    const u16* p = x + tok * 448 + 56 * c;
    res[0] = *(const v4u*)(p + 8 * gq);
    res[1] = *(const v4u*)(p + 8 * min(gq + 4, 6));
    asm volatile("" : "+v"(res[0]), "+v"(res[1]));
  }

#pragma clang loop unroll(disable)
  for (int l = 0; l < 2; ++l) {
    const unsigned LB = OFF_L + (unsigned)l * L_BYTES;
    FRESH_IDS();
    v4u qf[2];
    {
      const unsigned xo = (l == 0 ? OFF_X0 : OFF_L + L_X2) + (unsigned)T * 28 * KB;
      const unsigned so = OFF_L + L_S2 + (unsigned)T * 2048;
      f32x16 ak, av, aq;
      LnStat ls;
#pragma clang loop unroll(disable)
      for (int pass = 0;; ++pass) {
        asm volatile("" : "+s"(pass)::"memory");
        v4u xb[29];
        v2u st[4];
        x_enter(rs, xo, so, l != 0, lane, xb, st, [&]() {
          if (pass == 0) {
            VMWAIT(16);
            bar();
          }
        });
        gemm3<true>(L + R1 + lane16, rs, xo, lane16, xb, st, l != 0, h, ak, av, aq, ls);
        if (pass || !__any((ak[0] != ak[0]) | (ls.q != ls.q))) break;
        wait_valid(rs, lane16, xo, 28, KB);
      }
      STAMP(1 + 16 * l);
      LAS float* rb = (LAS float*)(L + R3) + w * 32;
      if (!h) rb[lane] = ls.r;
      v4u kv[4];
      kv[0] = clampS4(pack8(ak, 0, ls.r));
      kv[1] = clampS4(pack8(ak, 1, ls.r));
      const unsigned ko = LB + L_KV + (unsigned)((j * 64 + T) * 4) * KB;
      stc(rs, kv[0], lane16, ko);
      stc(rs, kv[1], lane16 + KB, ko);
      LGKM0();
#pragma unroll
      for (int i = 0; i < 4; ++i) {
        const f32x4 rr = *(LAS f32x4*)(rb + 8 * i + 4 * h);
#pragma unroll
        for (int e = 0; e < 4; ++e) av[4 * i + e] = ((lane & 31) == 28) ? 1.f : av[4 * i + e] * rr[e];
      }
      kv[2] = clampS4(pack8(av, 0, 1.f));
      kv[3] = clampS4(pack8(av, 1, 1.f));
      stc(rs, kv[2], lane16 + 2 * KB, ko);
      stc(rs, kv[3], lane16 + 3 * KB, ko);
      LAS unsigned char* kd = opq(L + R2 + (unsigned)((8 + w) * 4) * KB + lane16);
#pragma unroll
      for (int i = 0; i < 4; ++i) lds_wr(kd + i * KB, kv[i]);
      qf[0] = pack8(aq, 0, ls.r);
      qf[1] = pack8(aq, 1, ls.r);
      if (h) qf[1].z = 0x00003F80u;
    }
    VMWAIT(0);
    FRESH_IDS();
    {
      const int tq = 4 * g - 8 + 2 * w;
      {
        v4u kv[8];
        if (tq >= 0) {
          const unsigned ko = LB + L_KV + (unsigned)((j * 64 + tq) * 4) * KB;
#pragma clang loop unroll(disable)
          for (int tries = 0;; ++tries) {
            asm volatile("" : "+v"(lane16)::"memory");
            unsigned m = 0;
#pragma unroll
            for (int i = 0; i < 8; ++i) kv[i] = ldc(rs, lane16 + i * KB, ko);
#pragma unroll
            for (int i = 0; i < 8; ++i) m = mx4(m, kv[i]);
            if (!__any(m == SENT) || tries > TRY_CAP) break;
#pragma clang loop unroll(disable)
            for (int t3 = 0; t3 < TRY_CAP; ++t3) {
              __builtin_amdgcn_s_sleep(4);
              const unsigned pv = __builtin_amdgcn_raw_buffer_load_b32(rs, (lane & 1) * 4096 + 4092, ko, AUX_SC1);
              if (!__any(pv == SENT)) break;
            }
          }
        } else {
#pragma unroll
          for (int i = 0; i < 8; ++i) kv[i] = v4u{0, 0, 0, 0};
          kv[1].z = kv[5].z = h ? 0x0000F149u : 0u;
        }
        LAS unsigned char* kd = opq(L + R2 + (unsigned)(8 * w) * KB + lane16);
#pragma unroll
        for (int i = 0; i < 8; ++i) lds_wr(kd + i * KB, kv[i]);
      }
      bar();
      STAMP(2 + 16 * l);
      const unsigned char* g3 = ws + OFF_W3T + (unsigned)(c * 64 + 8 * w) * KB;
      const unsigned w4o = OFF_W4T + (unsigned)(j * 7) * 29 * KB;

      LAS unsigned char* kvb = opq(L + R2 + (unsigned)w * 4 * KB + lane16);
      const int iq = (lane & 31) - 4 * h;
      f32x16 s[9];
      float mx0 = -INFINITY, mx1 = -INFINITY;
      v4u kf[9][2];
      sfor<3>([&](auto I) {
        constexpr int i = I, t = (i == 0) ? 0 : (i == 1 ? 8 : i - 1);
        kf[t][0] = lds_rd(kvb + (t * 4 + 0) * KB);
        kf[t][1] = lds_rd(kvb + (t * 4 + 1) * KB);
      });
      GAP();
      sfor<10>([&](auto I) {
        constexpr int i = I;
        if constexpr (i < 9) {
          constexpr int t = (i == 0) ? 0 : (i == 1 ? 8 : i - 1);
          if constexpr (i == 0) mfn0(s[t], kf[t][0], qf[0]); else mf0(s[t], kf[t][0], qf[0]);
          mf(s[t], kf[t][1], qf[1]);
          if constexpr (i + 3 < 9) {
            constexpr int tn = i + 2;
            kf[tn][0] = lds_rd(kvb + (tn * 4 + 0) * KB);
            kf[tn][1] = lds_rd(kvb + (tn * 4 + 1) * KB);
          }
          if constexpr (i < 6) dma_share<87, 22, i>(ws, w4o, lbase + R1, w, lane16);
        }
        if constexpr (i >= 2) {
          constexpr int p = i - 1, t = (p == 1) ? 0 : p - 1;
          if constexpr (i >= 9) mfence(s[t]); else pin(s[t]);
          if constexpr (p == 1) {
            pin(s[8]);
#pragma unroll
            for (int r = 0; r < 16; ++r) s[0][r] = ((r & 3) + 8 * (r >> 2) <= iq) ? s[8][r] : s[0][r];
          }
#pragma unroll
          for (int r = 0; r < 16; r += 4) {
            mx0 = max3f(mx0, s[t][r], s[t][r + 1]);
            mx1 = max3f(mx1, s[t][r + 2], s[t][r + 3]);
          }
        }
        GAP();
      });
      v4u vf[9][2];
      vf[8][0] = lds_rd(kvb + (8 * 4 + 2) * KB);
      vf[8][1] = lds_rd(kvb + (8 * 4 + 3) * KB);
      vf[0][0] = lds_rd(kvb + (0 * 4 + 2) * KB);
      vf[0][1] = lds_rd(kvb + (0 * 4 + 3) * KB);
      float m = max3f(mx0, mx1, mx1);
      m = max3f(lower32(m), upper32(m), m);
      constexpr float SC = 0.18898223650461362f * 1.4426950408889634f;
      const float mc = -m * SC;
      f32x16 o;
      sfor<16>([&](auto I) {
        constexpr int i = I >> 1, hf = I & 1;
        if constexpr (i + 1 < 8) vf[i + 1][hf] = lds_rd(kvb + ((i + 1) * 4 + 2 + hf) * KB);
#pragma unroll
        for (int r = 8 * hf; r < 8 * hf + 8; ++r) s[i][r] = __builtin_amdgcn_exp2f(fmaf(s[i][r], SC, mc));
        if constexpr (i == 0) {
#pragma unroll
          for (int r = 8 * hf; r < 8 * hf + 8; ++r) {
            const bool d = ((r & 3) + 8 * (r >> 2) <= iq);
            s[8][r] = d ? s[0][r] : 0.f;
            s[0][r] = d ? 0.f : s[0][r];
          }
          if constexpr (hf == 0) mfn0(o, vf[8][0], pack8(s[8], 0, 1.f)); else mfn(o, vf[8][1], pack8(s[8], 1, 1.f));
        }
        mfn(o, vf[i][hf], pack8(s[i], hf, 1.f));
        dma_share<87, 22, 6 + I>(ws, w4o, lbase + R1, w, lane16);
        if (l == 0) lda1<I * 4, (I & 3) * 1024>(g3 + ((I >> 3) * 32 + (I & 4)) * KB, lane16);
        GAP();
      });
      mfence(o);
      const float li = __builtin_amdgcn_rcpf(upper32(o[12]));
      const unsigned oo = LB + L_OF + (unsigned)(T * 32 + 2 * j) * KB;
      stc(rs, clampS4(pack8(o, 0, li)), lane16, oo);
      stc(rs, clampS4(pack8(o, 1, li)), lane16 + KB, oo);
      STAMP(3 + 16 * l);
      VMWAIT(0);
      bar();
    }
    FRESH_IDS();
    {
      f32x16 acc[2][2];
      const unsigned oo = LB + L_OF + (unsigned)(T0 * 32 + 8 * w) * KB;
      const unsigned pvo = (unsigned)(((lane >> 2) & 1) * 32 + (lane & 3) * 2 + 1) * KB + 1020;
#pragma clang loop unroll(disable)
      for (int pass = 0;; ++pass) {
        asm volatile("" : "+s"(pass)::"memory");
        asm volatile("" : "+v"(lane16));
        v4u ob[2][8];
        const unsigned pv = __builtin_amdgcn_raw_buffer_load_b32(rs, pvo, oo, AUX_SC1);
#pragma unroll
        for (int k = 0; k < 8; ++k)
#pragma unroll
          for (int tt = 0; tt < 2; ++tt) ob[tt][k] = ldc(rs, lane16 + k * KB, oo + tt * 32 * KB);
        GAP();
        if (!__any(pv == SENT) || pass > 8) {
          sfor<8>([&](auto K) {
            constexpr int k = K;
            sfor<4>([&](auto Q) {
              constexpr int tt = Q >> 1, nt = Q & 1;
              if constexpr (k == 0) mfa0<(nt * 8 + k) * 4>(acc[tt][nt], ob[tt][k]); else mfa<(nt * 8 + k) * 4>(acc[tt][nt], ob[tt][k]);
            });
          });
          asm volatile("s_nop 7\n\ts_nop 3" : "+v"(acc[0][0]), "+v"(acc[0][1]), "+v"(acc[1][0]), "+v"(acc[1][1]));
          if (pass > 8 || !__any((acc[0][0][0] != acc[0][0][0]) | (acc[1][0][0] != acc[1][0][0]))) break;
          wait_valid(rs, lane16, oo, 8, KB);
          wait_valid(rs, lane16, oo + 32 * KB, 8, KB);
          pass = 8;
        } else {
#pragma clang loop unroll(disable)
          for (int t3 = 0; t3 < TRY_CAP; ++t3) {
            __builtin_amdgcn_s_sleep(4);
            const unsigned p2 = __builtin_amdgcn_raw_buffer_load_b32(rs, pvo, oo, AUX_SC1);
            if (!__any(p2 == SENT)) break;
          }
        }
      }
      STAMP(4 + 16 * l);
      FRESH_IDS();
      reduce_tile(L, acc, w, lane, res);
      publish_x(rs, LB + L_X1, LB + L_S1, T0, c, w, lane, res);
      bar();
      STAMP(5 + 16 * l);
      sfor<15>([&](auto I) { dma_share<58, 15, I>(ws, OFF_W4T + (unsigned)(j * 7 + 3) * 29 * KB, lbase + R2, w, lane16); });
    }
    FRESH_IDS();
    {
      const unsigned xo = LB + L_X1 + (unsigned)T * 28 * KB;
      const unsigned so = LB + L_S1 + (unsigned)T * 2048;
      v4u hb[7][4][2];
      const unsigned hi = LB + L_HF + (unsigned)(T0 * 112 + 28 * w) * KB;
      {
        const unsigned ho = LB + L_HF + (unsigned)(T * 112 + 7 * j) * KB + lane16;
        v4u xb[29];
        f32x16 a[7];
        LnStat ls;
#pragma clang loop unroll(disable)
        for (int pass = 0;; ++pass) {
          asm volatile("" : "+s"(pass)::"memory");
          v2u st[4];
          x_enter(rs, xo, so, true, lane, xb, st, [&]() {});
          gemm3<false>(L + R1 + lane16, rs, xo, lane16, xb, st, true, h, a[0], a[1], a[2], ls);
          if (pass || !__any((a[0][0] != a[0][0]) | (ls.q != ls.q))) break;
          wait_valid(rs, lane16, xo, 28, KB);
        }
        bar();
        STAMP(6 + 16 * l);
        const unsigned char* g5 = ws + OFF_W5T + (unsigned)(c * 224 + 28 * w) * KB;
        const unsigned w4o = OFF_W4T + (unsigned)(j * 7 + 5) * 29 * KB;
        float k1 = -2.f * 0.7978845608028654f * 1.4426950408889634f, k2 = k1 * 0.044715f;
        asm volatile("" : "+v"(k1), "+v"(k2));
        GeluSt gs;
        v4u hv;
        v4u ab[4];
        LAS unsigned char* WA = opq(L + R2 + lane16);
        LAS unsigned char* WB = opq(L + R1 + lane16);
        lane16 = opq(lane16);
        auto a_addr = [&](int q) -> LAS unsigned char* { return q < 58 ? WA + q * KB : WB + (q - 58) * KB; };
#pragma unroll
        for (int q = 0; q < 4; ++q) ab[q] = lds_rd(a_addr(q));
        GAP();
        sfor<140>([&](auto Q) {
          constexpr int q = Q;
          if constexpr (q < 116) {
            constexpr int t = 3 + q / 29, k = q % 29;
            if constexpr (k == 0) mf0(a[t], ab[q & 3], xb[0]); else mf(a[t], ab[q & 3], xb[k]);
            if constexpr (q == 54) {
              if (l == 0) VMWAIT(13); else VMWAIT(1);
              bar();
            }
            if constexpr (q + 4 < 116) ab[q & 3] = lds_rd(a_addr(q + 4));
            if constexpr (q < 30 && (q & 1) == 0) dma_share<58, 15, q / 2>(ws, w4o, lbase + R1, w, lane16);
            if constexpr (q >= 30 && ((q & 1) == 0 || q >= 105)) {
              constexpr int n = (q < 105) ? (q - 30) / 2 : 38 + (q - 105);
              if constexpr (n < 48) {
                constexpr int grp = n / 6, i = n % 6, jj = grp >> 1, nt = grp & 1;
                if (l == 0) lda1<64 + grp * 24 + i * 4, (i & 3) * 1024>(g5 + (nt * 112 + jj * 7 + (i & 4)) * KB, lane16);
              }
            }
          }
          if constexpr (q == 116) __builtin_amdgcn_s_barrier();
          constexpr int pr = q / 5, stg = q % 5, t = pr >> 2, pp = pr & 3;
          if constexpr (stg == 0 && pp == 0) {
            if constexpr (t == 6) mfence(a[6]); else pin(a[t]);
          }
          unsigned ov = 0;
          gelu_stage<stg>(gs, a[t][2 * pp], a[t][8 + 2 * pp], a[t][2 * pp + 1], a[t][8 + 2 * pp + 1], ls.r, ls.rinv, k1, k2, ov);
          if constexpr (stg == 4) {
            hv[pp] = ov;
            if constexpr (pp == 3) stc(rs, hv, ho + t * KB, 0);
          }
          if constexpr (q >= 116 && q < 132) {
            constexpr int n = q - 116, i = n >> 3, jj = (n >> 1) & 3, tt = n & 1;
            hb[i][jj][tt] = ldc(rs, lane16 + (jj * 7 + i) * KB, hi + tt * 112 * KB);
          }
          GAP();
        });
      }
      STAMP(7 + 16 * l);
      FRESH_IDS();

      f32x16 acc[2][2];
      v4u wl[4][2];
      sfor<16>([&](auto N) {
        constexpr int n = N + 16, i = n >> 3, jj = (n >> 1) & 3, tt = n & 1;
        hb[i][jj][tt] = ldc(rs, lane16 + (jj * 7 + i) * KB, hi + tt * 112 * KB);
      });
      GAP();
      sfor<7>([&](auto I) {
        constexpr int i = I;
        unsigned m = 0;
#pragma unroll
        for (int jj = 0; jj < 4; ++jj) m = mx4(mx4(m, hb[i][jj][0]), hb[i][jj][1]);
        if (__any(m == SENT)) {
#pragma clang loop unroll(disable)
          for (int tries = 0; tries < TRY_CAP; ++tries) {
            __builtin_amdgcn_s_sleep(8);
            asm volatile("" : "+v"(lane16)::"memory");
            m = 0;
#pragma unroll
            for (int jj = 0; jj < 4; ++jj)
#pragma unroll
              for (int tt = 0; tt < 2; ++tt) hb[i][jj][tt] = ldc(rs, lane16 + (jj * 7 + i) * KB, hi + tt * 112 * KB);
#pragma unroll
            for (int jj = 0; jj < 4; ++jj) m = mx4(mx4(m, hb[i][jj][0]), hb[i][jj][1]);
            if (!__any(m == SENT)) break;
          }
          asm volatile("s_nop 1");
        }
        lane16 = opq(lane16);
        sfor<16>([&](auto Q) {
          constexpr int jj = Q >> 2, tt = (Q >> 1) & 1, nt = Q & 1;
          if constexpr (i == 6) mf(acc[tt][nt], wl[jj][nt], hb[i][jj][tt]);
          else if constexpr (i == 0 && jj == 0) mfa0<64 + (jj * 2 + nt) * 24 + i * 4>(acc[tt][nt], hb[i][jj][tt]);
          else mfa<64 + (jj * 2 + nt) * 24 + i * 4>(acc[tt][nt], hb[i][jj][tt]);
          if constexpr (nt == 1) {
            if constexpr (i + 4 < 7) hb[i + 4][jj][tt] = ldc(rs, lane16 + (jj * 7 + i + 4) * KB, hi + tt * 112 * KB);
            if constexpr (i == 3) wl[jj][tt] = ldp(rs, lane16 + (jj * 7 + 6) * KB, OFF_W5T + (unsigned)(c * 224 + 28 * w + tt * 112) * KB);
          }
          GAP();
        });
      });
      asm volatile("s_nop 7\n\ts_nop 3" : "+v"(acc[0][0]), "+v"(acc[0][1]), "+v"(acc[1][0]), "+v"(acc[1][1]));
      STAMP(8 + 16 * l);
      FRESH_IDS();
      reduce_tile(L, acc, w, lane, res);
      if (l == 0) {
        publish_x(rs, OFF_L + L_X2, OFF_L + L_S2, T0, c, w, lane, res);
        sfor<22>([&](auto I) { dma_share<87, 22, I>(ws, OFF_W1T + (unsigned)j * 87 * KB, lbase + R1, w, lane16); });
      } else {
        const int tok = 32 * (T0 + (w >> 1)) + 16 * (w & 1) + (lane & 15), gq = lane >> 4;
        u16* p = out + tok * 448 + 56 * c;
        *(v4u*)(p + 8 * gq) = res[0];
        if (gq < 3) *(v4u*)(p + 8 * (gq + 4)) = res[1];
      }
      STAMP(9 + 16 * l);
    }
  }
}

}

extern "C" void kernel_launch(void* const* d_in, const int* in_sizes, int n_in, void* d_out, int out_size, void* d_ws, size_t ws_size,
                              hipStream_t stream) {
  const u16* x = (const u16*)d_in[0];
  const u16* wqkv = (const u16*)d_in[1];
  const u16* wo = (const u16*)d_in[2];
  const u16* wg = (const u16*)d_in[4];
  const u16* wu = (const u16*)d_in[5];
  const u16* wd = (const u16*)d_in[6];
  unsigned char* ws = (unsigned char*)d_ws;
  k_prep<<<256, 256, 0, stream>>>(x, wqkv, wo, wg, wu, wd, ws);
  k_main<<<256, 256, 0, stream>>>(x, (u16*)d_out, ws);
}
```

```cpp
#include <hip/hip_runtime.h>
#include <stdint.h>
#include <type_traits>

typedef unsigned short u16;
typedef short bf16x8 __attribute__((ext_vector_type(8)));
typedef short s16x4 __attribute__((ext_vector_type(4)));
typedef __bf16 bf16x2 __attribute__((ext_vector_type(2)));
typedef float f32x2 __attribute__((ext_vector_type(2)));
typedef float f32x4 __attribute__((ext_vector_type(4)));
typedef float f32x16 __attribute__((ext_vector_type(16)));
typedef unsigned v4u __attribute__((ext_vector_type(4)));
typedef unsigned v2u __attribute__((ext_vector_type(2)));
typedef __amdgpu_buffer_rsrc_t rsrc_t;
#define LAS __attribute__((address_space(3)))
#define DI __device__ __forceinline__

#define MAPV 1

namespace {

constexpr unsigned KB = 1024;
constexpr unsigned OFF_W1T = 0;
constexpr unsigned OFF_W4T = OFF_W1T + 48 * 29 * KB;
constexpr unsigned OFF_W5T = OFF_W4T + 112 * 29 * KB;
constexpr unsigned OFF_W3T = OFF_W5T + 16 * 112 * KB;
constexpr unsigned OFF_X0 = OFF_W3T + 16 * 32 * KB;
constexpr unsigned OFF_L = OFF_X0 + 64 * 28 * KB;
constexpr unsigned L_KV = 0;
constexpr unsigned L_OF = L_KV + 4096 * KB;
constexpr unsigned L_X1 = L_OF + 2048 * KB;
constexpr unsigned L_S1 = L_X1 + 1792 * KB;
constexpr unsigned L_HF = L_S1 + 128 * KB;
constexpr unsigned L_X2 = L_HF + 7168 * KB;
constexpr unsigned L_S2 = L_X2 + 1792 * KB;
constexpr unsigned L_BYTES = L_S2 + 128 * KB;
constexpr unsigned OFF_DBG = OFF_L + 2 * L_BYTES;
constexpr unsigned WS_BYTES = OFF_DBG + 1024 * KB;
constexpr unsigned SENT = 0xFFFFFFFFu;
constexpr int AUX_SC1 = 16;
constexpr int TRY_CAP = 20000;
constexpr unsigned MAIN_CODE_BYTES = 45 * 1024;
constexpr unsigned PREP_CODE_BYTES = 4 * 1024;
constexpr unsigned PREP_FILL = 6 * 1024 * KB;
constexpr unsigned OFF_FLAG = OFF_L + L_BYTES + L_S2;
constexpr unsigned OOB = 0x80000000u;

constexpr unsigned R1 = 0;
constexpr unsigned R2 = 87 * KB;
constexpr unsigned R3 = R2 + 58 * KB;
constexpr unsigned R4 = R3 + 1 * KB;
constexpr unsigned LDS_BYTES = R4 + 8 * KB;

DI unsigned pk(float a, float b) {
  f32x2 v = {a, b};
  bf16x2 r = __builtin_convertvector(v, bf16x2);
  return __builtin_bit_cast(unsigned, r);
}
DI float bflo(unsigned d) { return __uint_as_float(d << 16); }
DI float bfhi(unsigned d) { return __uint_as_float(d & 0xffff0000u); }
DI float rbf(float a) { return bflo(pk(a, a)); }
DI unsigned umax3(unsigned a, unsigned b, unsigned c) { return max(max(a, b), c); }
DI unsigned mx4(unsigned m, v4u v) { return umax3(umax3(m, v.x, v.y), v.z, v.w); }
DI unsigned clampS(unsigned d) { return min(d, 0xFFFFFFFEu); }
DI v4u clampS4(v4u v) { return v4u{clampS(v.x), clampS(v.y), clampS(v.z), clampS(v.w)}; }
DI float add32(float v) {
  unsigned u = __float_as_uint(v);
  auto r = __builtin_amdgcn_permlane32_swap(u, u, false, false);
  return __uint_as_float(r[0]) + __uint_as_float(r[1]);
}
DI float add16(float v) {
  unsigned u = __float_as_uint(v);
  auto r = __builtin_amdgcn_permlane16_swap(u, u, false, false);
  return __uint_as_float(r[0]) + __uint_as_float(r[1]);
}
DI float upper32(float v) {
  unsigned u = __float_as_uint(v);
  auto r = __builtin_amdgcn_permlane32_swap(u, u, false, false);
  return __uint_as_float(r[1]);
}
DI float lower32(float v) {
  unsigned u = __float_as_uint(v);
  auto r = __builtin_amdgcn_permlane32_swap(u, u, false, false);
  return __uint_as_float(r[0]);
}
DI v4u pack8(const f32x16& a, int s, float r) {
  return v4u{pk(a[8 * s] * r, a[8 * s + 1] * r), pk(a[8 * s + 2] * r, a[8 * s + 3] * r), pk(a[8 * s + 4] * r, a[8 * s + 5] * r),
             pk(a[8 * s + 6] * r, a[8 * s + 7] * r)};
}
DI v4u pack8n(const f32x16& a, int s) {
  return v4u{pk(a[8 * s], a[8 * s + 1]), pk(a[8 * s + 2], a[8 * s + 3]), pk(a[8 * s + 4], a[8 * s + 5]), pk(a[8 * s + 6], a[8 * s + 7])};
}
DI v4u ldc(rsrc_t rs, unsigned voff, unsigned soff) { return __builtin_amdgcn_raw_buffer_load_b128(rs, voff, soff, AUX_SC1); }
DI v2u ldc64(rsrc_t rs, unsigned voff, unsigned soff) { return __builtin_amdgcn_raw_buffer_load_b64(rs, voff, soff, AUX_SC1); }
DI unsigned ldc32(rsrc_t rs, unsigned voff, unsigned soff) { return __builtin_amdgcn_raw_buffer_load_b32(rs, voff, soff, AUX_SC1); }
DI v4u ldp(rsrc_t rs, unsigned voff, unsigned soff) { return __builtin_amdgcn_raw_buffer_load_b128(rs, voff, soff, 0); }
DI void stc(rsrc_t rs, v4u v, unsigned off) { __builtin_amdgcn_raw_buffer_store_b128(v, rs, off, 0, AUX_SC1); }
DI void stc64(rsrc_t rs, v2u v, unsigned off) { __builtin_amdgcn_raw_buffer_store_b64(v, rs, off, 0, AUX_SC1); }
DI v4u lds_rd(LAS unsigned char* p) { return *(LAS v4u*)p; }
DI void lds_wr(LAS unsigned char* p, v4u v) { *(LAS v4u*)p = v; }
DI unsigned opq(unsigned v) { asm volatile("" : "+v"(v)); return v; }
DI LAS unsigned char* opq(LAS unsigned char* p) { asm volatile("" : "+v"(p)); return p; }
#define VMWAIT(n) asm volatile("s_waitcnt vmcnt(%c0)" ::"i"(n) : "memory")
#define LGKM0() asm volatile("s_waitcnt lgkmcnt(0)" ::: "memory")
DI void bar() {
  LGKM0();
  __builtin_amdgcn_s_barrier();
  asm volatile("" ::: "memory");
}
DI void touch(const unsigned char* p, unsigned bytes, unsigned tid, unsigned nthr, unsigned la) {
  for (unsigned o = tid * 128; o < bytes; o += nthr * 128)
    asm volatile("s_mov_b32 m0, %2\n\ts_nop 0\n\tglobal_load_lds_dword %0, %1" ::"v"(o), "s"(p), "s"(la) : "memory");
}

#define STAMP(i)
#define COUNT(i)

#define ACLOB "a0","a1","a2","a3","a4","a5","a6","a7","a8","a9","a10","a11","a12","a13","a14","a15","a16","a17","a18","a19","a20","a21","a22","a23","a24","a25","a26","a27","a28","a29","a30","a31","a32","a33","a34","a35","a36","a37","a38","a39","a40","a41","a42","a43","a44","a45","a46","a47","a48","a49","a50","a51","a52","a53","a54","a55","a56","a57","a58","a59","a60","a61","a62","a63","a64","a65","a66","a67","a68","a69","a70","a71","a72","a73","a74","a75","a76","a77","a78","a79","a80","a81","a82","a83","a84","a85","a86","a87","a88","a89","a90","a91","a92","a93","a94","a95","a96","a97","a98","a99","a100","a101","a102","a103","a104","a105","a106","a107","a108","a109","a110","a111","a112","a113","a114","a115","a116","a117","a118","a119","a120","a121","a122","a123","a124","a125","a126","a127","a128","a129","a130","a131","a132","a133","a134","a135","a136","a137","a138","a139","a140","a141","a142","a143","a144","a145","a146","a147","a148","a149","a150","a151","a152","a153","a154","a155","a156","a157","a158","a159","a160","a161","a162","a163","a164","a165","a166","a167","a168","a169","a170","a171","a172","a173","a174","a175","a176","a177","a178","a179","a180","a181","a182","a183","a184","a185","a186","a187","a188","a189","a190","a191","a192","a193","a194","a195","a196","a197","a198","a199","a200","a201","a202","a203","a204","a205","a206","a207","a208","a209","a210","a211","a212","a213","a214","a215","a216","a217","a218","a219","a220","a221","a222","a223","a224","a225","a226","a227","a228","a229","a230","a231","a232","a233","a234","a235","a236","a237","a238","a239","a240","a241","a242","a243","a244","a245","a246","a247","a248","a249","a250","a251","a252","a253","a254","a255"
#define GAP() __builtin_amdgcn_sched_barrier(0)
#define MFMA_OP "v_mfma_f32_32x32x16_bf16 "
template <int N, int I = 0, class F> DI void sfor(F&& f) {
  if constexpr (I < N) {
    f(std::integral_constant<int, I>{});
    sfor<N, I + 1>(f);
  }
}
template <bool FIRST> DI void mf(f32x16& c, v4u a, v4u b) {
  if constexpr (FIRST) asm volatile(MFMA_OP "%0, %1, %2, 0" : "=&v"(c) : "v"(a), "v"(b) : ACLOB);
  else asm volatile(MFMA_OP "%0, %1, %2, %0" : "+v"(c) : "v"(a), "v"(b) : ACLOB);
}
template <bool FIRST> DI void mfn(f32x16& c, v4u a, v4u b) {
  if constexpr (FIRST) asm volatile("s_nop 1\n\t" MFMA_OP "%0, %1, %2, 0" : "=&v"(c) : "v"(a), "v"(b) : ACLOB);
  else asm volatile("s_nop 1\n\t" MFMA_OP "%0, %1, %2, %0" : "+v"(c) : "v"(a), "v"(b) : ACLOB);
}
template <bool FIRST, int A0, int A1> DI void mfa2(f32x16& c0, f32x16& c1, v4u b) {
  if constexpr (FIRST)
    asm volatile(MFMA_OP "%0, a[%c3:%c4], %2, 0\n\t" MFMA_OP "%1, a[%c5:%c6], %2, 0"
                 : "=&v"(c0), "=&v"(c1) : "v"(b), "i"(A0), "i"(A0 + 3), "i"(A1), "i"(A1 + 3) : ACLOB);
  else
    asm volatile(MFMA_OP "%0, a[%c3:%c4], %2, %0\n\t" MFMA_OP "%1, a[%c5:%c6], %2, %1"
                 : "+v"(c0), "+v"(c1) : "v"(b), "i"(A0), "i"(A0 + 3), "i"(A1), "i"(A1 + 3) : ACLOB);
}
DI void mf2(f32x16& c0, f32x16& c1, v4u a0, v4u a1, v4u b) {
  asm volatile(MFMA_OP "%0, %2, %4, %0\n\t" MFMA_OP "%1, %3, %4, %1" : "+v"(c0), "+v"(c1) : "v"(a0), "v"(a1), "v"(b) : ACLOB);
}
DI void pin(f32x16& c) { asm volatile("" : "+v"(c)); }
DI void mfence(f32x16& c) { asm volatile("s_nop 7\n\ts_nop 3" : "+v"(c)); }
DI v4u make_sent() {
  v4u v;
  asm volatile("v_mov_b32 %0, -1\n\tv_mov_b32 %1, -1\n\tv_mov_b32 %2, -1\n\tv_mov_b32 %3, -1" : "=v"(v.x), "=v"(v.y), "=v"(v.z), "=v"(v.w));
  return v;
}
DI void use(v4u v) { asm volatile("" ::"v"(v)); }
DI void set_m0(unsigned la) { asm volatile("s_mov_b32 m0, %0" ::"s"(la) : "memory"); }
template <int OFF> DI void dma_go(const unsigned char* g, unsigned voff) {
  asm volatile("global_load_lds_dwordx4 %0, %1 offset:%c2" ::"v"(voff), "s"(g), "i"(OFF) : "memory");
}
template <int I> DI void dma_piece(const unsigned char* g, unsigned la, unsigned lane16) {
  asm volatile("s_mov_b32 m0, %2\n\ts_nop 0\n\tglobal_load_lds_dwordx4 %0, %1 offset:%c3"
               ::"v"(lane16), "s"(g + (I & ~3) * KB), "s"(la + (I & ~3) * KB), "i"((I & 3) * 1024) : "memory");
}
template <int A, int OFF> DI void lda1(const unsigned char* g, unsigned voff) {
  asm volatile("global_load_dwordx4 a[%c2:%c3], %0, %1 offset:%c4" ::"v"(voff), "s"(g), "i"(A), "i"(A + 3), "i"(OFF) : "memory", ACLOB);
}
DI float max3f(float a, float b, float c) { float r; asm("v_max3_f32 %0, %1, %2, %3" : "=v"(r) : "v"(a), "v"(b), "v"(c)); return r; }
template <int R> DI void max8(float& m0, float& m1, const f32x16& s) {
  asm volatile("v_max3_f32 %0, %0, %2, %3\n\tv_max3_f32 %1, %1, %4, %5\n\tv_max3_f32 %0, %0, %6, %7\n\tv_max3_f32 %1, %1, %8, %9"
               : "+v"(m0), "+v"(m1)
               : "v"(s[R]), "v"(s[R + 1]), "v"(s[R + 2]), "v"(s[R + 3]), "v"(s[R + 4]), "v"(s[R + 5]), "v"(s[R + 6]), "v"(s[R + 7]));
}

DI void wait_valid(rsrc_t rs, unsigned voff, unsigned soff, int n, unsigned long long& dbg) {
  COUNT(0);
#pragma clang loop unroll(disable)
  for (int tries = 0; tries < TRY_CAP; ++tries) {
    unsigned m = 0;
#pragma unroll 4
    for (int i = 0; i < n; ++i) m = mx4(m, ldc(rs, voff, soff + (unsigned)i * KB));
    if (!__any(m == SENT)) break;
    __builtin_amdgcn_s_sleep(16);
  }
}
DI void poll32(rsrc_t rs, unsigned voff, unsigned soff, int site, unsigned long long& dbg) {
#pragma clang loop unroll(disable)
  for (int t = 0; t < TRY_CAP; ++t) {
    COUNT(site);
    __builtin_amdgcn_s_sleep(4);
    if (!__any(ldc32(rs, voff, soff) == SENT)) break;
  }
}

struct LnStat { float r, den, q; v4u corr; };

DI LnStat ln_stat(const v2u (&st)[4], bool raw, int h) {
  float s = (__uint_as_float(st[0].x) + __uint_as_float(st[1].x)) + (__uint_as_float(st[2].x) + __uint_as_float(st[3].x));
  float q = (__uint_as_float(st[0].y) + __uint_as_float(st[1].y)) + (__uint_as_float(st[2].y) + __uint_as_float(st[3].y));
  s = add32(s);
  q = add32(q);
  const float mean = s * (1.f / 448.f);
  const float var = fmaxf(q * (1.f / 448.f) - mean * mean, 0.f);
  const float den = rbf(__builtin_amdgcn_sqrtf(rbf(rbf(var) + 1e-5f)));
  LnStat o;
  o.q = raw ? q : 0.f;
  o.den = raw ? den : 1.f;
  o.r = raw ? __builtin_amdgcn_rcpf(den) : 1.f;
  const unsigned nm = pk(-mean, -mean);
  const unsigned nz = raw ? nm : 0u;
  o.corr = v4u{nz, nz, nz, nz};
  return o;
}
DI unsigned st_max(const v2u (&st)[4]) {
  return umax3(umax3(umax3(st[0].x, st[0].y, st[1].x), st[1].y, st[2].x), umax3(st[2].y, st[3].x, st[3].y), 0u);
}
constexpr int XD = 10;

template <class F>
DI void x_enter(rsrc_t rs, unsigned xo, unsigned so, bool raw, int lane, v4u (&xb)[29], LnStat& ls, unsigned long long& dbg, F&& once) {
#pragma clang loop unroll(disable)
  for (int t2 = 0;; ++t2) {
    asm volatile("" : "+s"(t2)::"memory");
    asm volatile("" : "+v"(lane));
    const unsigned sv = (unsigned)((lane >> 5) * 1024 + (lane & 31) * 8), l16 = lane * 16;
    v2u st[4];
#pragma unroll
    for (int i = 0; i < 4; ++i) st[i] = ldc64(rs, sv + i * 256, so);
#pragma unroll
    for (int k = 0; k < XD; ++k) xb[k] = ldc(rs, l16 + k * KB, xo);
    GAP();
    if (t2 == 0) once();
    ls = ln_stat(st, raw, lane >> 5);
    if (t2 || !raw || !__any(st_max(st) == SENT)) break;
    poll32(rs, (unsigned)((lane & 15) * 128 + 124), so, 1, dbg);
  }
  xb[28] = ls.corr;
}

template <int NT, bool SWAP1, class F>
DI void gemm_k(LAS unsigned char* W, rsrc_t rs, unsigned xo, unsigned lane16, v4u (&xb)[29], f32x16& a0, f32x16& a1, f32x16& a2, F&& hook) {
  v4u ab[3][2];
  W = opq(W);
  lane16 = opq(lane16);
  LAS unsigned char* W2 = opq(W + 58 * KB);
#pragma unroll
  for (int t = 0; t < 2; ++t) {
    ab[t][0] = lds_rd(W + (t * 29 + 0) * KB);
    ab[t][1] = lds_rd(W + (t * 29 + 1) * KB);
  }
  if constexpr (NT == 3) {
    ab[2][0] = lds_rd(W2);
    ab[2][1] = lds_rd(W2 + KB);
  }
  GAP();
  sfor<29>([&](auto K) {
    constexpr int k = K;
    mf<k == 0>(a0, ab[0][k & 1], xb[k]);
    if constexpr (k + 2 <= 28) ab[0][k & 1] = lds_rd(W + (0 * 29 + k + 2) * KB);
    if constexpr (k + XD < 28) xb[k + XD] = ldc(rs, lane16 + (k + XD) * KB, xo);
    hook(K);
    if constexpr (SWAP1) mf<k == 0>(a1, xb[k], ab[1][k & 1]); else mf<k == 0>(a1, ab[1][k & 1], xb[k]);
    if constexpr (k + 2 <= 28) ab[1][k & 1] = lds_rd(W + (1 * 29 + k + 2) * KB);
    if constexpr (NT == 3) {
      mf<k == 0>(a2, ab[2][k & 1], xb[k]);
      if constexpr (k + 2 <= 28) ab[2][k & 1] = lds_rd(W2 + (k + 2) * KB);
    }
    GAP();
  });
  if constexpr (NT == 3) asm volatile("s_nop 7\n\ts_nop 3" : "+v"(a0), "+v"(a1), "+v"(a2));
  else asm volatile("s_nop 7\n\ts_nop 3" : "+v"(a0), "+v"(a1));
}

template <int E> DI void red_write(LAS unsigned char* wp, const f32x16 (&acc)[2]) {
  const f32x16& a = acc[E >> 2];
  constexpr int i = E & 3;
  *(LAS f32x4*)(wp + E * KB) = f32x4{a[4 * i], a[4 * i + 1], a[4 * i + 2], a[4 * i + 3]};
}
DI void red_read(LAS unsigned char* L, int w, int lane, v4u (&o)[2]) {
  const int g = lane >> 4, tk = (w & 1) * 16 + (lane & 15), tt = w >> 1;
  LAS unsigned char* p0 = opq(L + R2 + (unsigned)(tt * 7 + g) * KB + tk * 16);
  LAS unsigned char* p1 = opq(L + R2 + (unsigned)(tt * 7 + min(g + 4, 6)) * KB + tk * 16);
  f32x4 res[2][2];
  LAS unsigned char* rp = opq(L + R4 + (unsigned)w * 2 * KB + lane * 16);
  o[0] = lds_rd(rp);
  o[1] = lds_rd(rp + KB);
#pragma unroll
  for (int rd = 0; rd < 2; ++rd) {
    res[rd][0] = f32x4{bflo(o[rd].x), bfhi(o[rd].x), bflo(o[rd].y), bfhi(o[rd].y)};
    res[rd][1] = f32x4{bflo(o[rd].z), bfhi(o[rd].z), bflo(o[rd].w), bfhi(o[rd].w)};
  }
  asm volatile("" : "+v"(res[0][0]), "+v"(res[0][1]), "+v"(res[1][0]), "+v"(res[1][1]));
  bar();
  f32x4 v[2][2][4];
#pragma unroll
  for (int rd = 0; rd < 2; ++rd)
#pragma unroll
    for (int kq = 0; kq < 4; ++kq) {
      LAS unsigned char* p = (rd ? p1 : p0) + kq * 14 * KB;
      v[rd][0][kq] = *(LAS f32x4*)p;
      v[rd][1][kq] = *(LAS f32x4*)(p + 512);
    }
  GAP();
#pragma unroll
  for (int rd = 0; rd < 2; ++rd) {
    const f32x4 lo = (res[rd][0] + (v[rd][0][0] + v[rd][0][1])) + (v[rd][0][2] + v[rd][0][3]);
    const f32x4 hi = (res[rd][1] + (v[rd][1][0] + v[rd][1][1])) + (v[rd][1][2] + v[rd][1][3]);
    o[rd] = clampS4(v4u{pk(lo[0], lo[1]), pk(lo[2], lo[3]), pk(hi[0], hi[1]), pk(hi[2], hi[3])});
  }
}
DI void res_keep(LAS unsigned char* L, int w, int lane, const v4u (&o)[2]) {
  LAS unsigned char* rp = opq(L + R4 + (unsigned)w * 2 * KB + lane * 16);
  lds_wr(rp, o[0]);
  lds_wr(rp + KB, o[1]);
}
DI float dot2(unsigned a, unsigned b, float c) {
  return __builtin_amdgcn_fdot2_f32_bf16(__builtin_bit_cast(bf16x2, a), __builtin_bit_cast(bf16x2, b), c, false);
}
struct PubOff { unsigned x0, x1, s; };
DI PubOff pub_off(int T0, int c, int w, int lane) {
  const int g = lane >> 4, tk = (w & 1) * 16 + (lane & 15), T = T0 + (w >> 1);
  PubOff p;
  p.x0 = (unsigned)((7 * c + g) * 512 + tk * 16) + (unsigned)T * 28 * KB;
  p.x1 = g < 3 ? p.x0 + 2048 : 0x80000000u;
  p.s = lane < 16 ? (unsigned)(tk * 8 + (T * 8 + c) * 256) + (L_S1 - L_X1) : 0x80000000u;
  return p;
}
DI void publish_x(rsrc_t rs, unsigned xoff, const PubOff& p, int lane, const v4u (&o)[2]) {
  stc(rs, o[0], p.x0 + xoff);
  stc(rs, o[1], p.x1 + xoff);
  constexpr unsigned ONE2 = 0x3F803F80u;
  float s = dot2(o[0].x, ONE2, 0.f), q = dot2(o[0].x, o[0].x, 0.f);
  float s1 = dot2(o[1].x, ONE2, 0.f), q1 = dot2(o[1].x, o[1].x, 0.f);
  s = dot2(o[0].y, ONE2, s); q = dot2(o[0].y, o[0].y, q); s1 = dot2(o[1].y, ONE2, s1); q1 = dot2(o[1].y, o[1].y, q1);
  s = dot2(o[0].z, ONE2, s); q = dot2(o[0].z, o[0].z, q); s1 = dot2(o[1].z, ONE2, s1); q1 = dot2(o[1].z, o[1].z, q1);
  s = dot2(o[0].w, ONE2, s); q = dot2(o[0].w, o[0].w, q); s1 = dot2(o[1].w, ONE2, s1); q1 = dot2(o[1].w, o[1].w, q1);
  const bool two = lane < 48;
  s += two ? s1 : 0.f;
  q += two ? q1 : 0.f;
  s = add32(add16(s));
  q = add32(add16(q));
  stc64(rs, v2u{clampS(__float_as_uint(s)), clampS(__float_as_uint(q))}, p.s + xoff);
}

struct GeluSt { float t0, t1, u0, u1; };
template <int STG> DI void gelu_stage(GeluSt& s, float g0, float u0, float g1, float u1, float c1, float c2, float d2, unsigned& out) {
  if constexpr (STG == 0) {
    asm volatile("v_mul_f32 %0, %2, %2\n\tv_mul_f32 %1, %3, %3\n\tv_fma_f32 %0, %0, %5, %4\n\tv_fma_f32 %1, %1, %5, %4"
                 : "=&v"(s.t0), "=&v"(s.t1) : "v"(g0), "v"(g1), "v"(c1), "v"(c2));
  } else if constexpr (STG == 1) {
    asm volatile("v_mul_f32 %0, %2, %0\n\tv_mul_f32 %1, %3, %1\n\tv_exp_f32 %0, %0" : "+v"(s.t0), "+v"(s.t1) : "v"(g0), "v"(g1));
  } else if constexpr (STG == 2) {
    asm volatile("v_exp_f32 %2, %2\n\tv_mul_f32 %0, %3, %4\n\tv_mul_f32 %1, %5, %6"
                 : "=&v"(s.u0), "=&v"(s.u1), "+v"(s.t1) : "v"(g0), "v"(u0), "v"(g1), "v"(u1));
  } else if constexpr (STG == 3) {
    asm volatile("v_fma_f32 %0, %0, %2, %2\n\tv_fma_f32 %1, %1, %2, %2\n\tv_rcp_f32 %0, %0" : "+v"(s.t0), "+v"(s.t1) : "v"(d2));
  } else {
    asm volatile("v_rcp_f32 %4, %4\n\tv_mul_f32 %1, %1, %3\n\tv_mul_f32 %2, %2, %4\n\tv_cvt_pk_bf16_f32 %0, %1, %2"
                 : "=&v"(out), "+v"(s.u0), "+v"(s.u1), "+v"(s.t0), "+v"(s.t1));
  }
}

}

extern "C" __global__ __launch_bounds__(256, 1) void k_main(const u16* __restrict__ x, u16* __restrict__ out, unsigned char* __restrict__ ws) {
  __shared__ __attribute__((aligned(16))) unsigned char smem[LDS_BYTES];
  LAS unsigned char* L = (LAS unsigned char*)smem;
  const unsigned lbase = (unsigned)(size_t)L;
  int b = blockIdx.x, lane = threadIdx.x & 63, w = __builtin_amdgcn_readfirstlane(threadIdx.x >> 6);
  int h, g, j, c, T0, T;
  unsigned lane16;
#define MAP_G(b) (((b) & 6) * 2 + ((b) >> 6))
#define MAP_J(b) ((((b) >> 2) & 14) | ((b) & 1))
#define FRESH_IDS()                  \
  do {                               \
    asm volatile("" : "+v"(lane));   \
    asm volatile("" : "+s"(w));      \
    asm volatile("" : "+s"(b));      \
    h = lane >> 5;                   \
    g = MAP_G(b);                    \
    j = MAP_J(b);                    \
    c = j >> 1;                      \
    T0 = 4 * g + 2 * (j & 1);        \
    T = 4 * g + w;                   \
    lane16 = lane * 16;              \
  } while (0)
  FRESH_IDS();
  const rsrc_t rs = __builtin_amdgcn_make_buffer_rsrc((void*)ws, 0, (int)WS_BYTES, 0x00020000);
  unsigned long long dbg = 0;
  STAMP(0);
  {
    unsigned long long pc;
    asm volatile("s_getpc_b64 %0" : "=s"(pc));
    touch((const unsigned char*)pc, MAIN_CODE_BYTES, threadIdx.x, 256, lbase + R3 + 512);
    const unsigned sh = (unsigned)min(w * 22, 65) * KB;
    sfor<22>([&](auto I) { dma_piece<I>(ws + OFF_W1T + (unsigned)j * 87 * KB + sh, lbase + R1 + sh, lane16); });
  }
  {
    const int tok = 32 * (T0 + (w >> 1)) + 16 * (w & 1) + (lane & 15), gq = lane >> 4;
    const unsigned xv = (unsigned)(tok * 448 + 56 * c) * 2;
    set_m0(lbase + R4 + (unsigned)w * 2 * KB);
    asm volatile("s_nop 0");
    dma_go<0>((const unsigned char*)x, xv + 16 * gq);
    set_m0(lbase + R4 + (unsigned)w * 2 * KB + KB);
    asm volatile("s_nop 0");
    dma_go<0>((const unsigned char*)x, xv + 16 * min(gq + 4, 6));
  }

#pragma clang loop unroll(disable)
  for (int l = 0; l < 2; ++l) {
    const unsigned LB = OFF_L + (unsigned)l * L_BYTES;
    FRESH_IDS();
    v4u qf[2];
    {
      const unsigned xo = (l == 0 ? OFF_X0 : OFF_L + L_X2) + (unsigned)T * 28 * KB;
      const unsigned so = OFF_L + L_S2 + (unsigned)T * 2048;
      f32x16 ak, av, aq;
      v4u xb[29];
      LnStat ls;
      unsigned kflag = 0;
#pragma clang loop unroll(disable)
      for (int pass = 0;; ++pass) {
        asm volatile("" : "+s"(pass)::"memory");
        x_enter(rs, xo, so, l != 0, lane, xb, ls, dbg, [&]() {
          if (pass == 0) {
            VMWAIT(4 + XD);
            bar();
          }
        });
        LAS float* rb = (LAS float*)(L + R3) + w * 32;
        rb[lane & 31] = ls.r;
        kflag = ldc32(rs, (lane & 1) * 128, OFF_FLAG + (unsigned)(j * 64 + max(4 * g - 8 + 2 * w, 0)) * 128);
        gemm_k<2, true>(L + R1 + lane16, rs, xo, lane16, xb, ak, av, aq, [](auto) {});
        if (pass || !__any((ak[0] != ak[0]) | (ls.q != ls.q))) break;
        wait_valid(rs, lane16, xo, 28, dbg);
      }
      STAMP(1 + 16 * l);
      FRESH_IDS();
      LAS float* rb = (LAS float*)(L + R3) + w * 32 + 4 * h;
      f32x4 rr[4];
#pragma unroll
      for (int i = 0; i < 4; ++i) rr[i] = *(LAS f32x4*)(rb + 8 * i);
      LAS unsigned char* WQ = opq(L + R1 + 58 * KB + lane16);
      v4u ab[4];
#pragma unroll
      for (int q = 0; q < 4; ++q) ab[q] = lds_rd(WQ + q * KB);
      v4u kv[4];
      const unsigned ko = LB + L_KV + (unsigned)((j * 64 + T) * 4) * KB + lane16;
      kv[0] = clampS4(pack8(ak, 0, ls.r));
      stc(rs, kv[0], ko);
      kv[1] = clampS4(pack8(ak, 1, ls.r));
      stc(rs, kv[1], ko + KB);
#pragma unroll
      for (int i = 0; i < 4; ++i)
#pragma unroll
        for (int e = 0; e < 4; ++e) av[4 * i + e] *= rr[i][e];
      kv[2] = clampS4(pack8n(av, 0));
      kv[3] = clampS4(pack8n(av, 1));
      if ((lane & 31) == 28) kv[2] = kv[3] = v4u{0x3F803F80u, 0x3F803F80u, 0x3F803F80u, 0x3F803F80u};
      stc(rs, kv[2], ko + 2 * KB);
      stc(rs, kv[3], ko + 3 * KB);
      LAS unsigned char* kd = opq(L + R2 + (unsigned)((8 + w) * 4) * KB + lane16);
#pragma unroll
      for (int i = 0; i < 4; ++i) lds_wr(kd + i * KB, kv[i]);
      __builtin_amdgcn_s_barrier();
      STAMP(10 + 16 * l);
      const unsigned sh = (unsigned)min(w * 15, 43) * KB;
      const unsigned char* g4 = ws + OFF_W4T + (unsigned)(j * 7) * 29 * KB + sh;
      const int tq = 4 * g - 8 + 2 * w;
      const unsigned kn = LB + L_KV + (unsigned)((j * 64 + max(tq, 0)) * 4) * KB;
      v4u nb[8];
      const PubOff pc1 = pub_off(T0, c, w, lane);
      const v4u sent4 = make_sent();
      if (l && tq >= 0 && __any(kflag == SENT)) poll32(rs, (lane & 1) * 128, OFF_FLAG + (unsigned)(j * 64 + tq) * 128, 7, dbg);
      GAP();
      sfor<29>([&](auto K) {
        constexpr int k = K;
        mf<k == 0>(aq, ab[k & 3], xb[k]);
        if constexpr (k + 4 < 29) ab[k & 3] = lds_rd(WQ + (k + 4) * KB);
        if constexpr ((k & 1) == 0 && k < 14) dma_piece<k / 2>(g4, lbase + R1 + sh, lane16);
        if constexpr (k == 3) stc(rs, sent4, pc1.x0 + LB + L_X1);
        if constexpr (k == 7) stc(rs, sent4, pc1.x1 + LB + L_X1);
        if constexpr (k == 11) stc64(rs, v2u{sent4.x, sent4.y}, pc1.s + LB + L_X1);
        if constexpr ((k & 1) == 0 && k >= 14) nb[(k - 14) / 2] = ldc(rs, lane16 + ((k - 14) / 2) * KB, kn);
        GAP();
      });
      mfence(aq);
      qf[0] = pack8(aq, 0, ls.r);
      qf[1] = pack8(aq, 1, ls.r);
      if (h) qf[1].z = 0x00003F80u;
      asm volatile("" : "+v"(qf[0]), "+v"(qf[1]));
      STAMP(11 + 16 * l);
      if (tq >= 0) {
        unsigned m = 0;
#pragma unroll
        for (int i = 0; i < 8; ++i) m = mx4(m, nb[i]);
        if (__any(m == SENT)) {
#pragma clang loop unroll(disable)
          for (int tries = 0; tries < 64; ++tries) {
            asm volatile("" : "+v"(lane16)::"memory");
            poll32(rs, (lane & 1) * 4096 + 4092, kn, 2, dbg);
            m = 0;
#pragma unroll
            for (int i = 0; i < 8; ++i) nb[i] = ldc(rs, lane16 + i * KB, kn);
#pragma unroll
            for (int i = 0; i < 8; ++i) m = mx4(m, nb[i]);
            if (!__any(m == SENT)) break;
          }
        }
      } else {
#pragma unroll
        for (int i = 0; i < 8; ++i) nb[i] = v4u{0, 0, 0, 0};
        nb[1].z = nb[5].z = h ? 0x0000F149u : 0u;
      }
      STAMP(12 + 16 * l);
      LAS unsigned char* kd2 = opq(L + R2 + (unsigned)(8 * w) * KB + lane16);
#pragma unroll
      for (int i = 0; i < 8; ++i) lds_wr(kd2 + i * KB, nb[i]);
    }
    FRESH_IDS();
    {
      VMWAIT(0);
      bar();
      STAMP(2 + 16 * l);
      const unsigned sh = (unsigned)(58 + min(w * 8, 21)) * KB, sh0 = (unsigned)min(w * 15, 43) * KB;
      const unsigned char* g4 = ws + OFF_W4T + (unsigned)(j * 7) * 29 * KB;

      LAS unsigned char* kvb = opq(L + R2 + (unsigned)w * 4 * KB + lane16);
      const int iq = (lane & 31) - 4 * h;
      f32x16 s[9];
      float mx0 = -INFINITY, mx1 = -INFINITY;
      v4u kf[9][2];
      v4u vf[9][2];
      sfor<2>([&](auto I) {
        constexpr int t = I * 8;
        kf[t][0] = lds_rd(kvb + (t * 4 + 0) * KB);
        kf[t][1] = lds_rd(kvb + (t * 4 + 1) * KB);
      });
      GAP();
      sfor<9>([&](auto I) {
        constexpr int i = I, t = (i == 0) ? 0 : (i == 1 ? 8 : i - 1), tn = i + 1;
        if constexpr (i == 0) mfn<true>(s[t], kf[t][0], qf[0]); else mf<true>(s[t], kf[t][0], qf[0]);
        if constexpr (tn < 8) kf[tn][0] = lds_rd(kvb + (tn * 4 + 0) * KB);
        if constexpr (i >= 4) max8<8>(mx0, mx1, s[i - 3]);
        if constexpr (i == 3) { max8<0>(mx0, mx1, s[0]); max8<8>(mx0, mx1, s[0]); }
        GAP();
        mf<false>(s[t], kf[t][1], qf[1]);
        if constexpr (tn < 8) kf[tn][1] = lds_rd(kvb + (tn * 4 + 1) * KB);
        if constexpr (i < 8) dma_piece<7 + i>(g4 + sh0, lbase + R1 + sh0, lane16);
        if constexpr (i == 2) {
          pin(s[0]);
          pin(s[8]);
#pragma unroll
          for (int r = 0; r < 16; ++r) s[0][r] = ((r & 3) + 8 * (r >> 2) <= iq) ? s[8][r] : s[0][r];
          pin(s[0]);
        }
        if constexpr (i >= 3) max8<0>(mx0, mx1, s[i - 2]);
        if constexpr (i == 7) { vf[8][0] = lds_rd(kvb + (8 * 4 + 2) * KB); vf[0][0] = lds_rd(kvb + (0 * 4 + 2) * KB); }
        if constexpr (i == 8) { vf[8][1] = lds_rd(kvb + (8 * 4 + 3) * KB); vf[0][1] = lds_rd(kvb + (0 * 4 + 3) * KB); }
        GAP();
      });
      max8<8>(mx0, mx1, s[6]);
      asm volatile("s_nop 7" : "+v"(s[7]));
      max8<0>(mx0, mx1, s[7]);
      max8<8>(mx0, mx1, s[7]);
      float m = max3f(mx0, mx1, mx1);
      m = max3f(lower32(m), upper32(m), m);
      constexpr float SC = 0.18898223650461362f * 1.4426950408889634f;
      const f32x2 sc2 = {SC, SC}, mc2 = {-m * SC, -m * SC};
      auto scale8 = [&](f32x16& v, int r0) {
#pragma unroll
        for (int r = r0; r < r0 + 8; r += 2) {
          const f32x2 z = __builtin_elementwise_fma(f32x2{v[r], v[r + 1]}, sc2, mc2);
          v[r] = z[0];
          v[r + 1] = z[1];
        }
      };
      f32x16 o;
      const unsigned char* g3 = ws + OFF_W3T + (unsigned)(c * 64 + 8 * w) * KB;
      const unsigned char* g5 = ws + OFF_W5T + (unsigned)(c * 224 + 28 * w) * KB;
      auto softmax_pv = [&](auto FIRST_LAYER) {
        constexpr bool L0 = FIRST_LAYER;
        v4u pf, pf8;
        const unsigned co = LB + L_HF + (unsigned)(T * 112 + 7 * j) * KB + lane16;
        const v4u sent4 = make_sent();
        scale8(s[0], 0);
        GAP();
        sfor<17>([&](auto N) {
          constexpr int n = N, i = n >> 1, hf = n & 1, pi = (n - 1) >> 1, ph = (n - 1) & 1;
          if constexpr (n >= 1) {
            if constexpr (pi == 0) mfn<n == 1>(o, vf[8][ph], pf8);
            if constexpr (pi == 0) mf<false>(o, vf[0][ph], pf); else mfn<false>(o, vf[pi][ph], pf);
          }
          if constexpr (n < 16) {
            auto bg = [&](auto SL) {
              constexpr int sl = SL;
              if constexpr (sl == 0) {
                if constexpr ((n & 1) == 0) dma_piece<n / 2>(g4 + sh, lbase + R1 + sh, lane16);
                else if constexpr (n < 14) stc(rs, sent4, co + (n / 2) * KB);
              } else if constexpr (L0) {
                constexpr int u = 2 * n + sl - 1;
                if constexpr (u < 16) lda1<u * 4, (u & 3) * 1024>(g3 + ((u >> 3) * 32 + (u & 4)) * KB, lane16);
                else {
                  constexpr int v = u - 16, grp = v / 6, e = v % 6, jj = grp >> 1, nt = grp & 1;
                  lda1<64 + grp * 24 + e * 4, (e & 3) * 1024>(g5 + (nt * 112 + jj * 7 + (e & 4)) * KB, lane16);
                }
              }
            };
            if constexpr (i + 1 < 8) vf[i + 1][hf] = lds_rd(kvb + ((i + 1) * 4 + 2 + hf) * KB);
            bg(std::integral_constant<int, 0>{});
            GAP();
#pragma unroll
            for (int r = 8 * hf; r < 8 * hf + 5; ++r) s[i][r] = __builtin_amdgcn_exp2f(s[i][r]);
            GAP();
            bg(std::integral_constant<int, 1>{});
            GAP();
#pragma unroll
            for (int r = 8 * hf + 5; r < 8 * hf + 8; ++r) s[i][r] = __builtin_amdgcn_exp2f(s[i][r]);
            GAP();
            if constexpr (n + 1 < 16) scale8(s[(n + 1) >> 1], 8 * ((n + 1) & 1));
            GAP();
            bg(std::integral_constant<int, 2>{});
            GAP();
            if constexpr (i == 0) {
#pragma unroll
              for (int r = 8 * hf; r < 8 * hf + 8; ++r) {
                const bool d = ((r & 3) + 8 * (r >> 2) <= iq);
                s[8][r] = d ? s[0][r] : 0.f;
                s[0][r] = d ? 0.f : s[0][r];
              }
              pf8 = pack8n(s[8], hf);
            }
            pf = pack8n(s[i], hf);
          }
          GAP();
        });
      };
      if (l == 0) softmax_pv(std::true_type{}); else softmax_pv(std::false_type{});
      mfence(o);
      const float li = __builtin_amdgcn_rcpf(upper32(o[12]));
      const unsigned oo = LB + L_OF + (unsigned)(T * 32 + 2 * j) * KB + lane16;
      stc(rs, clampS4(pack8(o, 0, li)), oo);
      stc(rs, clampS4(pack8(o, 1, li)), oo + KB);
      STAMP(3 + 16 * l);
      if (l == 0) {
        sfor<16>([&](auto Q) {
          constexpr int v = 16 + Q, grp = v / 6, e = v % 6, jj = grp >> 1, nt = grp & 1;
          lda1<64 + grp * 24 + e * 4, (e & 3) * 1024>(g5 + (nt * 112 + jj * 7 + (e & 4)) * KB, lane16);
        });
      } else {
        VMWAIT(0);
      }
      bar();
      STAMP(13 + 16 * l);
    }
    FRESH_IDS();
    {
      const unsigned oo = LB + L_OF + (unsigned)(T0 * 32 + 8 * w) * KB;
      f32x16 acc[2][2];
      v4u ob[16];
      auto o_load = [&](auto N) {
        constexpr int n = N;
        ob[n] = ldc(rs, lane16 + (n & 7) * KB, oo + (n >> 3) * 32 * KB);
      };
      sfor<10>(o_load);
      LAS unsigned char* wp = opq(L + R2 + (unsigned)(w * 14) * KB + lane16);
      unsigned m = mx4(mx4(mx4(mx4(0u, ob[0]), ob[1]), ob[2]), ob[3]);
      sfor<4>([&](auto B) {
        constexpr int bt = B;
        if (__any(m == SENT)) {
#pragma clang loop unroll(disable)
          for (int tries = 0; tries < TRY_CAP; ++tries) {
            COUNT(3);
            __builtin_amdgcn_s_sleep(6);
            asm volatile("" : "+v"(lane16)::"memory");
            sfor<4>([&](auto Q) { o_load(std::integral_constant<int, 4 * bt + Q>{}); });
            m = mx4(mx4(mx4(mx4(0u, ob[4 * bt]), ob[4 * bt + 1]), ob[4 * bt + 2]), ob[4 * bt + 3]);
            if (!__any(m == SENT)) break;
          }
          asm volatile("s_nop 1");
        }
        lane16 = opq(lane16);
        m = 0;
        sfor<4>([&](auto Q) {
          constexpr int n = 4 * bt + Q, tt = n >> 3, k = n & 7;
          mfa2<k == 0, k * 4, 32 + k * 4>(acc[tt][0], acc[tt][1], ob[n]);
          if constexpr (n + 10 < 16) o_load(std::integral_constant<int, n + 10>{});
          if constexpr (n + 4 < 16) m = mx4(m, ob[n + 4]);
          if constexpr (tt == 1 && k >= 1) {
            if constexpr (k == 1) { pin(acc[0][0]); pin(acc[0][1]); }
            red_write<k - 1>(wp, acc[0]);
          }
          GAP();
        });
      });
      asm volatile("s_nop 7\n\ts_nop 3" : "+v"(acc[1][0]), "+v"(acc[1][1]));
      sfor<7>([&](auto E) { red_write<E>(wp + 7 * KB, acc[1]); });
      STAMP(4 + 16 * l);
      FRESH_IDS();
      const PubOff po = pub_off(T0, c, w, lane);
      v4u res[2];
      red_read(L, w, lane, res);
      publish_x(rs, LB + L_X1, po, lane, res);
      res_keep(L, w, lane, res);
      __builtin_amdgcn_s_barrier();
      STAMP(5 + 16 * l);
      const unsigned sh = (unsigned)min(w * 15, 43) * KB;
      sfor<15>([&](auto I) { dma_piece<I>(ws + OFF_W4T + (unsigned)(j * 7 + 3) * 29 * KB + sh, lbase + R2 + sh, lane16); });
    }
    FRESH_IDS();
    {
      const unsigned xo = LB + L_X1 + (unsigned)T * 28 * KB;
      const unsigned so = LB + L_S1 + (unsigned)T * 2048;
      v4u hb[7][4][2];
      const unsigned hi = LB + L_HF + (unsigned)(T0 * 112 + 28 * w) * KB;
      auto h_load = [&](auto N) {
        constexpr int n = N, i = n >> 3, jj = (n >> 1) & 3, tt = n & 1;
        hb[i][jj][tt] = ldc(rs, lane16 + (jj * 7 + i) * KB, hi + tt * 112 * KB);
      };
      {
        v4u xb[29];
        f32x16 a[7];
        LnStat ls;
#pragma clang loop unroll(disable)
        for (int pass = 0;; ++pass) {
          asm volatile("" : "+s"(pass)::"memory");
          STAMP(14 + 16 * l);
          x_enter(rs, xo, so, true, lane, xb, ls, dbg, [&]() {});
          STAMP(15 + 16 * l);
          const PubOff pc2 = pub_off(T0, c, w, lane);
          const unsigned x2o = l ? OOB : OFF_L + L_X2;
          v4u sent4;
          gemm_k<3, false>(L + R1 + lane16, rs, xo, lane16, xb, a[0], a[1], a[2], [&](auto K) {
            constexpr int k = K;
            if constexpr (k == 19) sent4 = make_sent();
            if constexpr (k == 20) stc(rs, sent4, (pc2.x0 + x2o) | (pc2.x0 & OOB));
            if constexpr (k == 23) stc(rs, sent4, (pc2.x1 + x2o) | (pc2.x1 & OOB));
            if constexpr (k == 26) stc64(rs, v2u{sent4.x, sent4.y}, (pc2.s + x2o) | (pc2.s & OOB));
          });
          if (pass || !__any((a[0][0] != a[0][0]) | (ls.q != ls.q))) break;
          wait_valid(rs, lane16, xo, 28, dbg);
        }
        __builtin_amdgcn_s_barrier();
        STAMP(6 + 16 * l);
        const unsigned ho = LB + L_HF + (unsigned)(T * 112 + 7 * j) * KB + lane16;
        const unsigned sh = (unsigned)min(w * 15, 43) * KB;
        const unsigned char* g4 = ws + OFF_W4T + (unsigned)(j * 7 + 5) * 29 * KB + sh;
        const unsigned char* g5 = ws + OFF_W5T + (unsigned)(c * 224 + 28 * w) * KB;
        constexpr float K1 = -2.f * 0.7978845608028654f * 1.4426950408889634f, K2 = K1 * 0.044715f;
        float c1 = K1 * ls.r, c2 = K2 * ls.r * ls.r * ls.r, d2 = ls.den * ls.den;
        asm volatile("" : "+v"(c1), "+v"(c2), "+v"(d2));
        GeluSt gs;
        v4u hv, sent4;
        const unsigned nk = (l ? OOB : OFF_L + L_BYTES + L_KV + (unsigned)((j * 64 + T) * 4) * KB) + lane16;
        const unsigned no = (l ? OOB : OFF_L + L_BYTES + L_OF + (unsigned)(T * 32 + 2 * j) * KB) + lane16;
        v4u ab[6];
        LAS unsigned char* WA = opq(L + R2 + lane16);
        LAS unsigned char* WB = opq(L + R1 + lane16);
        lane16 = opq(lane16);
        auto a_addr = [&](int q) -> LAS unsigned char* { return q < 58 ? WA + q * KB : WB + (q - 58) * KB; };
#pragma unroll
        for (int q = 0; q < 6; ++q) ab[q] = lds_rd(a_addr(q));
        GAP();
        sfor<140>([&](auto Q) {
          constexpr int q = Q;
          constexpr bool dma = q < 30 && q % 2 == 0;
          if constexpr (q < 116) {
            constexpr int t = 3 + q / 29, k = q % 29;
            if constexpr ((q & 1) == 0) use(ab[(q + 1) % 6]);
            if constexpr (dma) set_m0(lbase + R1 + sh + ((q / 2) & ~3) * KB);
            mf<k == 0>(a[t], ab[q % 6], xb[k]);
            if constexpr (dma) dma_go<((q / 2) & 3) * 1024>(g4 + ((q / 2) & ~3) * KB, lane16);
            if constexpr (q == 52) {
              VMWAIT(4);
              bar();
            }
            if constexpr (q + 6 < 116) ab[q % 6] = lds_rd(a_addr(q + 6));
            if constexpr (q == 31) sent4 = make_sent();
            if constexpr (q >= 33 && q < 81 && (q - 33) % 8 == 0) {
              constexpr int z = (q - 33) / 8;
              if constexpr (z < 4) stc(rs, sent4, nk + z * KB); else stc(rs, sent4, no + (z - 4) * KB);
            }
            if constexpr (q >= 88 && (q & 1) == 0) h_load(std::integral_constant<int, (q - 88) / 2>{});
            if constexpr (q >= 54 && q < 86 && (q & 3) == 2) {
              if (l == 0) {
                sfor<2>([&](auto Z) {
                  constexpr int v = 32 + (q - 54) / 2 + Z, grp = v / 6, e = v % 6, jj = grp >> 1, nt = grp & 1;
                  lda1<64 + grp * 24 + e * 4, (e & 3) * 1024>(g5 + (nt * 112 + jj * 7 + (e & 4)) * KB, lane16);
                });
              }
            }
          }
          if constexpr (q == 116) __builtin_amdgcn_s_barrier();
          constexpr int pr = q / 5, stg = q % 5, t = pr >> 2, pp = pr & 3;
          if constexpr (stg == 0 && pp == 0) pin(a[t]);
          unsigned ov = 0;
          gelu_stage<stg>(gs, a[t][2 * pp], a[t][8 + 2 * pp], a[t][2 * pp + 1], a[t][8 + 2 * pp + 1], c1, c2, d2, ov);
          if constexpr (stg == 4) {
            hv[pp] = ov;
            if constexpr (pp == 3) stc(rs, hv, ho + t * KB);
          }
          if constexpr (q >= 116 && q < 140 && (q % 3) == 2) h_load(std::integral_constant<int, 14 + (q - 116) / 3>{});
          GAP();
        });
      }
      STAMP(7 + 16 * l);
      FRESH_IDS();

      f32x16 acc[2][2];
      v4u wl[4][2];
      LAS unsigned char* wp = opq(L + R2 + (unsigned)(w * 14) * KB + lane16);
      const unsigned sh1 = (unsigned)min(w * 22, 65) * KB;
      const unsigned char* g1 = ws + OFF_W1T + (unsigned)j * 87 * KB + sh1;
      unsigned m = 0;
#pragma unroll
      for (int jj = 0; jj < 4; ++jj) m = mx4(mx4(m, hb[0][jj][0]), hb[0][jj][1]);
      sfor<7>([&](auto I) {
        constexpr int i = I;
        if (__any(m == SENT)) {
#pragma clang loop unroll(disable)
          for (int tries = 0; tries < TRY_CAP; ++tries) {
            COUNT(i < 3 ? 4 : (i < 5 ? 5 : 6));
            __builtin_amdgcn_s_sleep(8);
            asm volatile("" : "+v"(lane16)::"memory");
            m = 0;
            sfor<8>([&](auto Q) { h_load(std::integral_constant<int, 8 * i + Q>{}); });
#pragma unroll
            for (int jj = 0; jj < 4; ++jj) m = mx4(mx4(m, hb[i][jj][0]), hb[i][jj][1]);
            if (!__any(m == SENT)) break;
          }
          asm volatile("s_nop 1");
        }
        lane16 = opq(lane16);
        m = 0;
        sfor<8>([&](auto Q) {
          if constexpr (i == 6) {
            constexpr int tt = Q >> 2, jj = Q & 3;
            mf2(acc[tt][0], acc[tt][1], wl[jj][0], wl[jj][1], hb[i][jj][tt]);
            if constexpr (Q == 5) { pin(acc[0][0]); pin(acc[0][1]); }
            if constexpr (Q >= 5) { red_write<2 * (Q - 5)>(wp, acc[0]); red_write<2 * (Q - 5) + 1>(wp, acc[0]); }
          } else {
            constexpr int jj = Q >> 1, tt = Q & 1, n = 22 + 8 * i + Q;
            mfa2<(i == 0 && jj == 0), 64 + (jj * 2) * 24 + i * 4, 64 + (jj * 2 + 1) * 24 + i * 4>(acc[tt][0], acc[tt][1], hb[i][jj][tt]);
            if constexpr (n < 56) h_load(std::integral_constant<int, n>{});
            else if constexpr (n < 64) {
              constexpr int z = n - 56;
              wl[z >> 1][z & 1] = ldp(rs, lane16 + ((z >> 1) * 7 + 6) * KB, OFF_W5T + (unsigned)(c * 224 + 28 * w + (z & 1) * 112) * KB);
            }
            m = mx4(m, hb[i + 1][jj][tt]);
          }
          GAP();
        });
      });
      red_write<6>(wp, acc[0]);
      asm volatile("s_nop 7\n\ts_nop 3" : "+v"(acc[1][0]), "+v"(acc[1][1]));
      sfor<7>([&](auto E) { red_write<E>(wp + 7 * KB, acc[1]); });
      STAMP(8 + 16 * l);
      FRESH_IDS();
      v4u res[2];
      if (l == 0) {
        sfor<8>([&](auto I) { dma_piece<I>(g1, lbase + R1 + sh1, lane16); });
        const PubOff po = pub_off(T0, c, w, lane);
        red_read(L, w, lane, res);
        publish_x(rs, OFF_L + L_X2, po, lane, res);
        if (lane == 0) __builtin_amdgcn_raw_buffer_store_b32(0u, rs, OFF_FLAG + (unsigned)(j * 64 + T) * 128, 0, AUX_SC1);
        res_keep(L, w, lane, res);
        sfor<14>([&](auto I) { dma_piece<8 + I>(g1, lbase + R1 + sh1, lane16); });
      } else {
        const int tok = 32 * (T0 + (w >> 1)) + 16 * (w & 1) + (lane & 15), gq = lane >> 4;
        u16* p = out + tok * 448 + 56 * c;
        red_read(L, w, lane, res);
        *(v4u*)(p + 8 * gq) = res[0];
        if (gq < 3) *(v4u*)(p + 8 * (gq + 4)) = res[1];
      }
      STAMP(9 + 16 * l);
    }
  }
}

template <int CTRL> DI float dpp_add(float v) {
  return v + __builtin_bit_cast(float, __builtin_amdgcn_update_dpp(0, __builtin_bit_cast(int, v), CTRL, 0xf, 0xf, true));
}
DI float sum32(float v) {
  v = dpp_add<0xB1>(v);
  v = dpp_add<0x4E>(v);
  v = dpp_add<0x141>(v);
  v = dpp_add<0x140>(v);
  return add16(v);
}
extern "C" __global__ __launch_bounds__(256, 2) void k_prep(const u16* __restrict__ x, const u16* __restrict__ wqkv, const u16* __restrict__ wo,
                                                           const u16* __restrict__ wg, const u16* __restrict__ wu, const u16* __restrict__ wd,
                                                           unsigned char* __restrict__ ws) {
  __shared__ __attribute__((aligned(16))) unsigned char smem[116 * 256 + 256];
  LAS unsigned char* img = (LAS unsigned char*)smem;
  const unsigned ibase = (unsigned)(size_t)img, sink = ibase + 116 * 256;
  const int role = (blockIdx.x >> 3) & 1, b = (blockIdx.x & 7) | ((blockIdx.x >> 4) << 3);
  const int tid = threadIdx.x, lane = tid & 63, w = __builtin_amdgcn_readfirstlane(tid >> 6);
  const int h = lane >> 5;
  const rsrc_t rs = __builtin_amdgcn_make_buffer_rsrc((void*)ws, 0, (int)WS_BYTES, 0x00020000);
#define PSTAMP(i)
  PSTAMP(0);
  if (role == 0) {
    const u16 *pA, *pB;
    int stride, kind, rq = b & 3;
    unsigned dst;
    if (b < 48) {
      const int part = b / 16, hg = (b >> 2) & 3;
      pA = wqkv + (part == 0 ? 448 : (part == 1 ? 896 : 0)) + 112 * hg;
      pB = pA + 64; stride = 1344; kind = 0;
      dst = OFF_W1T + (unsigned)(((4 * hg + w) * 3 + part) * 29) * KB;
    } else if (b < 160) {
      const int cg = (b - 48) >> 2;
      pA = wg + 64 * cg; pB = wu + 64 * cg; stride = 1792; kind = 1;
      dst = OFF_W4T + (unsigned)((4 * cg + w) * 29) * KB;
    } else if (b < 224) {
      const int i = b - 160, cg = i & 3;
      rq = i >> 2;
      pA = wd + 112 * cg; pB = pA + 64; stride = 448; kind = 2;
      dst = OFF_W5T + (unsigned)((4 * cg + w) * 112) * KB;
    } else {
      const int cg = ((b - 224) >> 2) & 3;
      pA = wo + 112 * cg; pB = pA + 64; stride = 448; kind = 3;
      dst = OFF_W3T + (unsigned)((4 * cg + w) * 32) * KB;
    }
    {
      const int r4 = lane >> 4, ch = (lane & 15) ^ (r4 << 2);
      const int chc = (kind == 1) ? ch : min(ch, 13);
      const u16* src = (chc < 8 ? pA : pB) + 8 * (chc & 7) + (112 * rq + 28 * w + r4) * stride;
#pragma unroll
      for (int i = 0; i < 7; ++i) {
        asm volatile("s_mov_b32 m0, %1\n\ts_nop 0\n\tglobal_load_lds_dwordx4 %0, off" ::"v"(src + 4 * i * stride), "s"(ibase + (unsigned)(28 * w + 4 * i) * 256)
                     : "memory");
      }
    }
    {
      unsigned long long pc;
      int delta;
      asm volatile("s_getpc_b64 %0\n\ts_mov_b32 %1, k_prep@rel32@lo+4" : "=s"(pc), "=s"(delta));
      touch((const unsigned char*)pc + delta, PREP_CODE_BYTES, tid, 256, sink);
    }
    PSTAMP(1);
    const int un = ((lane >> 4) & 1) * 4 + (lane & 3), q = (lane & 15) >> 2;
    int col;
    bool valid = true;
    if (kind == 0) { col = 28 * w + 4 * min(un, 6); valid = (lane & 31) < 28; }
    else if (kind == 1) col = (un < 4 ? 0 : 64) + 16 * w + 4 * (un & 3);
    else { col = 56 * (w >> 1) + 32 * (w & 1) + 4 * un; valid = !(w & 1) || (lane & 31) < 24; col = min(col, 108); }
    const int dlo = (kind < 2) ? 8 * h : 4 * h, dhi = (kind < 2) ? 4 : 8;
    LAS unsigned char* base = img + (dlo + q) * 256 + (((col >> 3) ^ (q << 2)) << 4) + (col & 4) * 2;
    const int nfr = (kind == 3) ? 8 : 7;
    const unsigned keep = valid ? 0xFFFFFFFFu : 0u;
    const unsigned dv = dst + (unsigned)(nfr * rq) * KB + lane * 16;
    f32x16 cs;
#pragma unroll
    for (int i = 0; i < 16; ++i) cs[i] = 0.f;
    const v4u ones = {0x3F803F80u, 0x3F803F80u, 0x3F803F80u, 0x3F803F80u};
    VMWAIT(0);
    __builtin_amdgcn_s_barrier();
    PSTAMP(2);
    if (b < 240) {
#pragma unroll
      for (int f = 0; f < 8; ++f) {
        if (f < nfr) {
          const int rb = (kind == 3) ? 28 * (f >> 1) + 16 * (f & 1) : 16 * f;
          s16x4 lo = __builtin_amdgcn_ds_read_tr16_b64_v4i16((LAS s16x4*)(base + rb * 256));
          s16x4 hi = __builtin_amdgcn_ds_read_tr16_b64_v4i16((LAS s16x4*)(base + (rb + dhi) * 256));
          const v2u l2 = __builtin_bit_cast(v2u, lo), h2 = __builtin_bit_cast(v2u, hi);
          const unsigned kh = (kind == 3 && (f & 1) && h) ? 0u : keep;
          const v4u fr = {l2.x & keep, l2.y & keep, h2.x & kh, h2.y & kh};
          stc(rs, fr, dv + f * KB);
          cs = __builtin_amdgcn_mfma_f32_32x32x16_bf16(__builtin_bit_cast(bf16x8, ones), __builtin_bit_cast(bf16x8, fr), cs, 0, 0, 0);
        }
      }
      if (kind < 2 && !h) {
        const float c = cs[0], c0 = rbf(c), c1 = rbf(c - c0), c2 = rbf(c - c0 - c1);
        stc64(rs, v2u{pk(c0, c1), pk(c2, 0.f)}, dst + 28 * KB + (unsigned)((rq >> 1) * 512 + lane * 16 + (rq & 1) * 8));
      }
    }
    PSTAMP(3);
  } else {
    const int row = 8 * b + 2 * w + h, li = lane & 31;
    const v4u* p = (const v4u*)(x + row * 448 + min(li, 27) * 16);
    const v4u xa = p[0], xb = p[1];
    GAP();
    {
      unsigned long long pc;
      int delta;
      asm volatile("s_getpc_b64 %0\n\ts_mov_b32 %1, k_main@rel32@lo+4" : "=s"(pc), "=s"(delta));
      if (w == 3 && b < 8) touch((const unsigned char*)pc + delta, MAIN_CODE_BYTES, b * 64 + lane, 512, sink);
    }
    {
      const v4u s4 = {SENT, SENT, SENT, SENT};
      constexpr int PER = PREP_FILL / (1024 * KB);
#pragma unroll 1
      for (int i = 0; i < PER; ++i) stc(rs, s4, OFF_L + (unsigned)((b * 4 + w) * PER + i) * KB + lane * 16);
      if (lane < 8) stc(rs, s4, OFF_FLAG + (unsigned)(b * 4 + w) * 128 + lane * 16);
    }
    GAP();
    PSTAMP(1);
    float v[16];
    const unsigned d[8] = {xa.x, xa.y, xa.z, xa.w, xb.x, xb.y, xb.z, xb.w};
#pragma unroll
    for (int i = 0; i < 8; ++i) { v[2 * i] = bflo(d[i]); v[2 * i + 1] = bfhi(d[i]); }
    float s = 0.f;
#pragma unroll
    for (int i = 0; i < 16; ++i) s += v[i];
    s = sum32(li < 28 ? s : 0.f);
    const float mean = s / 448.f;
    float q = 0.f;
#pragma unroll
    for (int i = 0; i < 16; ++i) { const float e = v[i] - mean; q += e * e; }
    q = sum32(li < 28 ? q : 0.f);
    const float mb = rbf(mean), den = rbf(sqrtf(rbf(rbf(q / 448.f) + 1e-5f)));
    const float r = 1.f / den;
    unsigned o8[8];
#pragma unroll
    for (int i = 0; i < 8; ++i) {
      const unsigned dd = pk(v[2 * i] - mb, v[2 * i + 1] - mb);
      const float d0 = bflo(dd), d1 = bfhi(dd);
      const float q0 = d0 * r, q1 = d1 * r;
      o8[i] = pk(fmaf(fmaf(-q0, den, d0), r, q0), fmaf(fmaf(-q1, den, d1), r, q1));
    }
    if (li < 28) {
      unsigned char* po = ws + OFF_X0 + (unsigned)((row >> 5) * 28 + li) * KB + (row & 31) * 16;
      *(v4u*)po = v4u{o8[0], o8[1], o8[2], o8[3]};
      *(v4u*)(po + 512) = v4u{o8[4], o8[5], o8[6], o8[7]};
    }
    PSTAMP(2);
    PSTAMP(3);
  }
}

extern "C" void kernel_launch(void* const* d_in, const int* in_sizes, int n_in, void* d_out, int out_size, void* d_ws, size_t ws_size,
                              hipStream_t stream) {
  const u16* x = (const u16*)d_in[0];
  const u16* wqkv = (const u16*)d_in[1];
  const u16* wo = (const u16*)d_in[2];
  const u16* wg = (const u16*)d_in[4];
  const u16* wu = (const u16*)d_in[5];
  const u16* wd = (const u16*)d_in[6];
  unsigned char* ws = (unsigned char*)d_ws;
  k_prep<<<512, 256, 0, stream>>>(x, wqkv, wo, wg, wu, wd, ws);
  k_main<<<256, 256, 0, stream>>>(x, (u16*)d_out, ws);
}
```

```cpp
#include <hip/hip_runtime.h>
#include <stdint.h>
#include <type_traits>

typedef unsigned short u16;
typedef short bf16x8 __attribute__((ext_vector_type(8)));
typedef short s16x4 __attribute__((ext_vector_type(4)));
typedef __bf16 bf16x2 __attribute__((ext_vector_type(2)));
typedef float f32x2 __attribute__((ext_vector_type(2)));
typedef float f32x4 __attribute__((ext_vector_type(4)));
typedef float f32x16 __attribute__((ext_vector_type(16)));
typedef unsigned v4u __attribute__((ext_vector_type(4)));
typedef unsigned v2u __attribute__((ext_vector_type(2)));
typedef __amdgpu_buffer_rsrc_t rsrc_t;
#define LAS __attribute__((address_space(3)))
#define DI __device__ __forceinline__

namespace {

constexpr unsigned KB = 1024;
constexpr unsigned OFF_W1T = 0;
constexpr unsigned OFF_W4T = OFF_W1T + 48 * 29 * KB;
constexpr unsigned OFF_W5T = OFF_W4T + 112 * 29 * KB;
constexpr unsigned OFF_W3T = OFF_W5T + 16 * 112 * KB;
constexpr unsigned OFF_X0 = OFF_W3T + 16 * 32 * KB;
constexpr unsigned OFF_L = OFF_X0 + 64 * 28 * KB;
constexpr unsigned L_KV = 0;
constexpr unsigned L_OF = L_KV + 4096 * KB;
constexpr unsigned L_X1 = L_OF + 2048 * KB;
constexpr unsigned L_S1 = L_X1 + 1792 * KB;
constexpr unsigned L_HF = L_S1 + 128 * KB;
constexpr unsigned L_X2 = L_HF + 7168 * KB;
constexpr unsigned L_S2 = L_X2 + 1792 * KB;
constexpr unsigned L_BYTES = L_S2 + 128 * KB;
constexpr unsigned WS_BYTES = OFF_L + 2 * L_BYTES;
constexpr unsigned SENT = 0xFFFFFFFFu;
constexpr int AUX_SC1 = 16;
constexpr int TRY_CAP = 20000;
constexpr unsigned PREP_FILL = 6 * 1024 * KB;
constexpr unsigned OFF_FLAG = OFF_L + L_BYTES + L_S2;
constexpr unsigned OOB = 0x80000000u;

constexpr unsigned R1 = 0;
constexpr unsigned R2 = 87 * KB;
constexpr unsigned R3 = R2 + 58 * KB;
constexpr unsigned R4 = R3 + 1 * KB;
constexpr unsigned LDS_BYTES = R4 + 8 * KB;

DI unsigned pk(float a, float b) {
  f32x2 v = {a, b};
  bf16x2 r = __builtin_convertvector(v, bf16x2);
  return __builtin_bit_cast(unsigned, r);
}
DI float bflo(unsigned d) { return __uint_as_float(d << 16); }
DI float bfhi(unsigned d) { return __uint_as_float(d & 0xffff0000u); }
DI float rbf(float a) { return bflo(pk(a, a)); }
DI unsigned umax3(unsigned a, unsigned b, unsigned c) { return max(max(a, b), c); }
DI unsigned mx4(unsigned m, v4u v) { return umax3(umax3(m, v.x, v.y), v.z, v.w); }
DI unsigned clampS(unsigned d) { return min(d, 0xFFFFFFFEu); }
DI v4u clampS4(v4u v) { return v4u{clampS(v.x), clampS(v.y), clampS(v.z), clampS(v.w)}; }
DI float add32(float v) {
  unsigned u = __float_as_uint(v);
  auto r = __builtin_amdgcn_permlane32_swap(u, u, false, false);
  return __uint_as_float(r[0]) + __uint_as_float(r[1]);
}
DI float add16(float v) {
  unsigned u = __float_as_uint(v);
  auto r = __builtin_amdgcn_permlane16_swap(u, u, false, false);
  return __uint_as_float(r[0]) + __uint_as_float(r[1]);
}
DI float upper32(float v) {
  unsigned u = __float_as_uint(v);
  auto r = __builtin_amdgcn_permlane32_swap(u, u, false, false);
  return __uint_as_float(r[1]);
}
DI float lower32(float v) {
  unsigned u = __float_as_uint(v);
  auto r = __builtin_amdgcn_permlane32_swap(u, u, false, false);
  return __uint_as_float(r[0]);
}
DI v4u pack8(const f32x16& a, int s, float r) {
  return v4u{pk(a[8 * s] * r, a[8 * s + 1] * r), pk(a[8 * s + 2] * r, a[8 * s + 3] * r), pk(a[8 * s + 4] * r, a[8 * s + 5] * r),
             pk(a[8 * s + 6] * r, a[8 * s + 7] * r)};
}
DI v4u pack8n(const f32x16& a, int s) {
  return v4u{pk(a[8 * s], a[8 * s + 1]), pk(a[8 * s + 2], a[8 * s + 3]), pk(a[8 * s + 4], a[8 * s + 5]), pk(a[8 * s + 6], a[8 * s + 7])};
}
DI v4u ldc(rsrc_t rs, unsigned voff, unsigned soff) { return __builtin_amdgcn_raw_buffer_load_b128(rs, voff, soff, AUX_SC1); }
DI v2u ldc64(rsrc_t rs, unsigned voff, unsigned soff) { return __builtin_amdgcn_raw_buffer_load_b64(rs, voff, soff, AUX_SC1); }
DI unsigned ldc32(rsrc_t rs, unsigned voff, unsigned soff) { return __builtin_amdgcn_raw_buffer_load_b32(rs, voff, soff, AUX_SC1); }
DI v4u ldp(rsrc_t rs, unsigned voff, unsigned soff) { return __builtin_amdgcn_raw_buffer_load_b128(rs, voff, soff, 0); }
DI void stc(rsrc_t rs, v4u v, unsigned off) { __builtin_amdgcn_raw_buffer_store_b128(v, rs, off, 0, AUX_SC1); }
DI void stc64(rsrc_t rs, v2u v, unsigned off) { __builtin_amdgcn_raw_buffer_store_b64(v, rs, off, 0, AUX_SC1); }
DI v4u lds_rd(LAS unsigned char* p) { return *(LAS v4u*)p; }
DI void lds_wr(LAS unsigned char* p, v4u v) { *(LAS v4u*)p = v; }
DI unsigned opq(unsigned v) { asm volatile("" : "+v"(v)); return v; }
DI LAS unsigned char* opq(LAS unsigned char* p) { asm volatile("" : "+v"(p)); return p; }
#define VMWAIT(n) asm volatile("s_waitcnt vmcnt(%c0)" ::"i"(n) : "memory")
#define LGKM0() asm volatile("s_waitcnt lgkmcnt(0)" ::: "memory")
DI void bar() {
  LGKM0();
  __builtin_amdgcn_s_barrier();
  asm volatile("" ::: "memory");
}
DI void touch(const unsigned char* p, unsigned bytes, unsigned tid, unsigned nthr, unsigned la) {
  for (unsigned o = tid * 128; o < bytes; o += nthr * 128)
    asm volatile("s_mov_b32 m0, %2\n\ts_nop 0\n\tglobal_load_lds_dword %0, %1" ::"v"(o), "s"(p), "s"(la) : "memory");
}

#define TOUCH_CODE(FROM, TO, tid, nthr, la)                                                                      \
  do {                                                                                                           \
    unsigned long long pc_;                                                                                      \
    int d0_, d1_;                                                                                                \
    asm volatile("s_getpc_b64 %0\n\ts_mov_b32 %1, " FROM "@rel32@lo+4\n\ts_mov_b32 %2, " TO "@rel32@lo+12"      \
                 : "=s"(pc_), "=s"(d0_), "=s"(d1_));                                                             \
    touch((const unsigned char*)pc_ + d0_, (unsigned)max(min(d1_ - d0_, 65536), 0), tid, nthr, la);              \
  } while (0)

#define ACLOB "a0","a1","a2","a3","a4","a5","a6","a7","a8","a9","a10","a11","a12","a13","a14","a15","a16","a17","a18","a19","a20","a21","a22","a23","a24","a25","a26","a27","a28","a29","a30","a31","a32","a33","a34","a35","a36","a37","a38","a39","a40","a41","a42","a43","a44","a45","a46","a47","a48","a49","a50","a51","a52","a53","a54","a55","a56","a57","a58","a59","a60","a61","a62","a63","a64","a65","a66","a67","a68","a69","a70","a71","a72","a73","a74","a75","a76","a77","a78","a79","a80","a81","a82","a83","a84","a85","a86","a87","a88","a89","a90","a91","a92","a93","a94","a95","a96","a97","a98","a99","a100","a101","a102","a103","a104","a105","a106","a107","a108","a109","a110","a111","a112","a113","a114","a115","a116","a117","a118","a119","a120","a121","a122","a123","a124","a125","a126","a127","a128","a129","a130","a131","a132","a133","a134","a135","a136","a137","a138","a139","a140","a141","a142","a143","a144","a145","a146","a147","a148","a149","a150","a151","a152","a153","a154","a155","a156","a157","a158","a159","a160","a161","a162","a163","a164","a165","a166","a167","a168","a169","a170","a171","a172","a173","a174","a175","a176","a177","a178","a179","a180","a181","a182","a183","a184","a185","a186","a187","a188","a189","a190","a191","a192","a193","a194","a195","a196","a197","a198","a199","a200","a201","a202","a203","a204","a205","a206","a207","a208","a209","a210","a211","a212","a213","a214","a215","a216","a217","a218","a219","a220","a221","a222","a223","a224","a225","a226","a227","a228","a229","a230","a231","a232","a233","a234","a235","a236","a237","a238","a239","a240","a241","a242","a243","a244","a245","a246","a247","a248","a249","a250","a251","a252","a253","a254","a255"
#define GAP() __builtin_amdgcn_sched_barrier(0)
#define MFMA_OP "v_mfma_f32_32x32x16_bf16 "
template <int N, int I = 0, class F> DI void sfor(F&& f) {
  if constexpr (I < N) {
    f(std::integral_constant<int, I>{});
    sfor<N, I + 1>(f);
  }
}
template <bool FIRST> DI void mf(f32x16& c, v4u a, v4u b) {
  if constexpr (FIRST) asm volatile(MFMA_OP "%0, %1, %2, 0" : "=&v"(c) : "v"(a), "v"(b) : ACLOB);
  else asm volatile(MFMA_OP "%0, %1, %2, %0" : "+v"(c) : "v"(a), "v"(b) : ACLOB);
}
template <bool FIRST> DI void mfn(f32x16& c, v4u a, v4u b) {
  if constexpr (FIRST) asm volatile("s_nop 1\n\t" MFMA_OP "%0, %1, %2, 0" : "=&v"(c) : "v"(a), "v"(b) : ACLOB);
  else asm volatile("s_nop 1\n\t" MFMA_OP "%0, %1, %2, %0" : "+v"(c) : "v"(a), "v"(b) : ACLOB);
}
template <bool FIRST, int A0, int A1> DI void mfa2(f32x16& c0, f32x16& c1, v4u b) {
  if constexpr (FIRST)
    asm volatile(MFMA_OP "%0, a[%c3:%c4], %2, 0\n\t" MFMA_OP "%1, a[%c5:%c6], %2, 0"
                 : "=&v"(c0), "=&v"(c1) : "v"(b), "i"(A0), "i"(A0 + 3), "i"(A1), "i"(A1 + 3) : ACLOB);
  else
    asm volatile(MFMA_OP "%0, a[%c3:%c4], %2, %0\n\t" MFMA_OP "%1, a[%c5:%c6], %2, %1"
                 : "+v"(c0), "+v"(c1) : "v"(b), "i"(A0), "i"(A0 + 3), "i"(A1), "i"(A1 + 3) : ACLOB);
}
DI void mf2(f32x16& c0, f32x16& c1, v4u a0, v4u a1, v4u b) {
  asm volatile(MFMA_OP "%0, %2, %4, %0\n\t" MFMA_OP "%1, %3, %4, %1" : "+v"(c0), "+v"(c1) : "v"(a0), "v"(a1), "v"(b) : ACLOB);
}
DI void pin(f32x16& c) { asm volatile("" : "+v"(c)); }
DI void mfence(f32x16& c) { asm volatile("s_nop 7\n\ts_nop 3" : "+v"(c)); }
DI v4u make_sent() {
  v4u v;
  asm volatile("v_mov_b32 %0, -1\n\tv_mov_b32 %1, -1\n\tv_mov_b32 %2, -1\n\tv_mov_b32 %3, -1" : "=v"(v.x), "=v"(v.y), "=v"(v.z), "=v"(v.w));
  return v;
}
DI void use(v4u v) { asm volatile("" ::"v"(v)); }
DI void set_m0(unsigned la) { asm volatile("s_mov_b32 m0, %0" ::"s"(la) : "memory"); }
template <int OFF> DI void dma_go(const unsigned char* g, unsigned voff) {
  asm volatile("global_load_lds_dwordx4 %0, %1 offset:%c2" ::"v"(voff), "s"(g), "i"(OFF) : "memory");
}
template <int I> DI void dma_piece(const unsigned char* g, unsigned la, unsigned lane16) {
  asm volatile("s_mov_b32 m0, %2\n\ts_nop 0\n\tglobal_load_lds_dwordx4 %0, %1 offset:%c3"
               ::"v"(lane16), "s"(g + (I & ~3) * KB), "s"(la + (I & ~3) * KB), "i"((I & 3) * 1024) : "memory");
}
template <int A, int OFF> DI void lda1(const unsigned char* g, unsigned voff) {
  asm volatile("global_load_dwordx4 a[%c2:%c3], %0, %1 offset:%c4" ::"v"(voff), "s"(g), "i"(A), "i"(A + 3), "i"(OFF) : "memory", ACLOB);
}
DI float max3f(float a, float b, float c) { float r; asm("v_max3_f32 %0, %1, %2, %3" : "=v"(r) : "v"(a), "v"(b), "v"(c)); return r; }
template <int R> DI void max8(float& m0, float& m1, const f32x16& s) {
  asm volatile("v_max3_f32 %0, %0, %2, %3\n\tv_max3_f32 %1, %1, %4, %5\n\tv_max3_f32 %0, %0, %6, %7\n\tv_max3_f32 %1, %1, %8, %9"
               : "+v"(m0), "+v"(m1)
               : "v"(s[R]), "v"(s[R + 1]), "v"(s[R + 2]), "v"(s[R + 3]), "v"(s[R + 4]), "v"(s[R + 5]), "v"(s[R + 6]), "v"(s[R + 7]));
}

DI void wait_valid(rsrc_t rs, unsigned voff, unsigned soff, int n) {
#pragma clang loop unroll(disable)
  for (int tries = 0; tries < TRY_CAP; ++tries) {
    unsigned m = 0;
#pragma unroll 4
    for (int i = 0; i < n; ++i) m = mx4(m, ldc(rs, voff, soff + (unsigned)i * KB));
    if (!__any(m == SENT)) break;
    __builtin_amdgcn_s_sleep(16);
  }
}
DI void poll32(rsrc_t rs, unsigned voff, unsigned soff) {
#pragma clang loop unroll(disable)
  for (int t = 0; t < TRY_CAP; ++t) {
    __builtin_amdgcn_s_sleep(4);
    if (!__any(ldc32(rs, voff, soff) == SENT)) break;
  }
}

struct LnStat { float r, den, q; v4u corr; };

DI LnStat ln_stat(const v2u (&st)[4], bool raw, int h) {
  float s = (__uint_as_float(st[0].x) + __uint_as_float(st[1].x)) + (__uint_as_float(st[2].x) + __uint_as_float(st[3].x));
  float q = (__uint_as_float(st[0].y) + __uint_as_float(st[1].y)) + (__uint_as_float(st[2].y) + __uint_as_float(st[3].y));
  s = add32(s);
  q = add32(q);
  const float mean = s * (1.f / 448.f);
  const float var = fmaxf(q * (1.f / 448.f) - mean * mean, 0.f);
  const float den = rbf(__builtin_amdgcn_sqrtf(rbf(rbf(var) + 1e-5f)));
  LnStat o;
  o.q = raw ? q : 0.f;
  o.den = raw ? den : 1.f;
  o.r = raw ? __builtin_amdgcn_rcpf(den) : 1.f;
  const unsigned nm = pk(-mean, -mean);
  const unsigned nz = raw ? nm : 0u;
  o.corr = v4u{nz, nz, nz, nz};
  return o;
}
DI unsigned st_max(const v2u (&st)[4]) {
  return umax3(umax3(umax3(st[0].x, st[0].y, st[1].x), st[1].y, st[2].x), umax3(st[2].y, st[3].x, st[3].y), 0u);
}
constexpr int XD = 10;

template <class F>
DI void x_enter(rsrc_t rs, unsigned xo, unsigned so, bool raw, int lane, v4u (&xb)[29], LnStat& ls, F&& once) {
#pragma clang loop unroll(disable)
  for (int t2 = 0;; ++t2) {
    asm volatile("" : "+s"(t2)::"memory");
    asm volatile("" : "+v"(lane));
    const unsigned sv = (unsigned)((lane >> 5) * 1024 + (lane & 31) * 8), l16 = lane * 16;
    v2u st[4];
#pragma unroll
    for (int i = 0; i < 4; ++i) st[i] = ldc64(rs, sv + i * 256, so);
#pragma unroll
    for (int k = 0; k < XD; ++k) xb[k] = ldc(rs, l16 + k * KB, xo);
    GAP();
    if (t2 == 0) once();
    ls = ln_stat(st, raw, lane >> 5);
    if (t2 || !raw || !__any(st_max(st) == SENT)) break;
    poll32(rs, (unsigned)((lane & 15) * 128 + 124), so);
  }
  xb[28] = ls.corr;
}

template <int NT, bool SWAP1, class F>
DI void gemm_k(LAS unsigned char* W, rsrc_t rs, unsigned xo, unsigned lane16, v4u (&xb)[29], f32x16& a0, f32x16& a1, f32x16& a2, F&& hook) {
  v4u ab[3][2];
  W = opq(W);
  lane16 = opq(lane16);
  LAS unsigned char* W2 = opq(W + 58 * KB);
#pragma unroll
  for (int t = 0; t < 2; ++t) {
    ab[t][0] = lds_rd(W + (t * 29 + 0) * KB);
    ab[t][1] = lds_rd(W + (t * 29 + 1) * KB);
  }
  if constexpr (NT == 3) {
    ab[2][0] = lds_rd(W2);
    ab[2][1] = lds_rd(W2 + KB);
  }
  GAP();
  sfor<29>([&](auto K) {
    constexpr int k = K;
    mf<k == 0>(a0, ab[0][k & 1], xb[k]);
    if constexpr (k + 2 <= 28) ab[0][k & 1] = lds_rd(W + (0 * 29 + k + 2) * KB);
    if constexpr (k + XD < 28) xb[k + XD] = ldc(rs, lane16 + (k + XD) * KB, xo);
    hook(K);
    if constexpr (SWAP1) mf<k == 0>(a1, xb[k], ab[1][k & 1]); else mf<k == 0>(a1, ab[1][k & 1], xb[k]);
    if constexpr (k + 2 <= 28) ab[1][k & 1] = lds_rd(W + (1 * 29 + k + 2) * KB);
    if constexpr (NT == 3) {
      mf<k == 0>(a2, ab[2][k & 1], xb[k]);
      if constexpr (k + 2 <= 28) ab[2][k & 1] = lds_rd(W2 + (k + 2) * KB);
    }
    GAP();
  });
  if constexpr (NT == 3) asm volatile("s_nop 7\n\ts_nop 3" : "+v"(a0), "+v"(a1), "+v"(a2));
  else asm volatile("s_nop 7\n\ts_nop 3" : "+v"(a0), "+v"(a1));
}

template <int E> DI void red_write(LAS unsigned char* wp, const f32x16 (&acc)[2]) {
  const f32x16& a = acc[E >> 2];
  constexpr int i = E & 3;
  *(LAS f32x4*)(wp + E * KB) = f32x4{a[4 * i], a[4 * i + 1], a[4 * i + 2], a[4 * i + 3]};
}
DI void red_read(LAS unsigned char* L, int w, int lane, v4u (&o)[2]) {
  const int g = lane >> 4, tk = (w & 1) * 16 + (lane & 15), tt = w >> 1;
  LAS unsigned char* p0 = opq(L + R2 + (unsigned)(tt * 7 + g) * KB + tk * 16);
  LAS unsigned char* p1 = opq(L + R2 + (unsigned)(tt * 7 + min(g + 4, 6)) * KB + tk * 16);
  f32x4 res[2][2];
  LAS unsigned char* rp = opq(L + R4 + (unsigned)w * 2 * KB + lane * 16);
  o[0] = lds_rd(rp);
  o[1] = lds_rd(rp + KB);
#pragma unroll
  for (int rd = 0; rd < 2; ++rd) {
    res[rd][0] = f32x4{bflo(o[rd].x), bfhi(o[rd].x), bflo(o[rd].y), bfhi(o[rd].y)};
    res[rd][1] = f32x4{bflo(o[rd].z), bfhi(o[rd].z), bflo(o[rd].w), bfhi(o[rd].w)};
  }
  asm volatile("" : "+v"(res[0][0]), "+v"(res[0][1]), "+v"(res[1][0]), "+v"(res[1][1]));
  bar();
  f32x4 v[2][2][4];
#pragma unroll
  for (int rd = 0; rd < 2; ++rd)
#pragma unroll
    for (int kq = 0; kq < 4; ++kq) {
      LAS unsigned char* p = (rd ? p1 : p0) + kq * 14 * KB;
      v[rd][0][kq] = *(LAS f32x4*)p;
      v[rd][1][kq] = *(LAS f32x4*)(p + 512);
    }
  GAP();
#pragma unroll
  for (int rd = 0; rd < 2; ++rd) {
    const f32x4 lo = (res[rd][0] + (v[rd][0][0] + v[rd][0][1])) + (v[rd][0][2] + v[rd][0][3]);
    const f32x4 hi = (res[rd][1] + (v[rd][1][0] + v[rd][1][1])) + (v[rd][1][2] + v[rd][1][3]);
    o[rd] = clampS4(v4u{pk(lo[0], lo[1]), pk(lo[2], lo[3]), pk(hi[0], hi[1]), pk(hi[2], hi[3])});
  }
}
DI void res_keep(LAS unsigned char* L, int w, int lane, const v4u (&o)[2]) {
  LAS unsigned char* rp = opq(L + R4 + (unsigned)w * 2 * KB + lane * 16);
  lds_wr(rp, o[0]);
  lds_wr(rp + KB, o[1]);
}
DI float dot2(unsigned a, unsigned b, float c) {
  return __builtin_amdgcn_fdot2_f32_bf16(__builtin_bit_cast(bf16x2, a), __builtin_bit_cast(bf16x2, b), c, false);
}
struct PubOff { unsigned x0, x1, s; };
DI PubOff pub_off(int T0, int c, int w, int lane) {
  const int g = lane >> 4, tk = (w & 1) * 16 + (lane & 15), T = T0 + (w >> 1);
  PubOff p;
  p.x0 = (unsigned)((7 * c + g) * 512 + tk * 16) + (unsigned)T * 28 * KB;
  p.x1 = g < 3 ? p.x0 + 2048 : 0x80000000u;
  p.s = lane < 16 ? (unsigned)(tk * 8 + (T * 8 + c) * 256) + (L_S1 - L_X1) : 0x80000000u;
  return p;
}
DI void publish_x(rsrc_t rs, unsigned xoff, const PubOff& p, int lane, const v4u (&o)[2]) {
  stc(rs, o[0], p.x0 + xoff);
  stc(rs, o[1], p.x1 + xoff);
  constexpr unsigned ONE2 = 0x3F803F80u;
  float s = dot2(o[0].x, ONE2, 0.f), q = dot2(o[0].x, o[0].x, 0.f);
  float s1 = dot2(o[1].x, ONE2, 0.f), q1 = dot2(o[1].x, o[1].x, 0.f);
  s = dot2(o[0].y, ONE2, s); q = dot2(o[0].y, o[0].y, q); s1 = dot2(o[1].y, ONE2, s1); q1 = dot2(o[1].y, o[1].y, q1);
  s = dot2(o[0].z, ONE2, s); q = dot2(o[0].z, o[0].z, q); s1 = dot2(o[1].z, ONE2, s1); q1 = dot2(o[1].z, o[1].z, q1);
  s = dot2(o[0].w, ONE2, s); q = dot2(o[0].w, o[0].w, q); s1 = dot2(o[1].w, ONE2, s1); q1 = dot2(o[1].w, o[1].w, q1);
  const bool two = lane < 48;
  s += two ? s1 : 0.f;
  q += two ? q1 : 0.f;
  s = add32(add16(s));
  q = add32(add16(q));
  stc64(rs, v2u{clampS(__float_as_uint(s)), clampS(__float_as_uint(q))}, p.s + xoff);
}

struct GeluSt { float t0, t1, u0, u1; };
template <int STG> DI void gelu_stage(GeluSt& s, float g0, float u0, float g1, float u1, float c1, float c2, float d2, unsigned& out) {
  if constexpr (STG == 0) {
    asm volatile("v_mul_f32 %0, %2, %2\n\tv_mul_f32 %1, %3, %3\n\tv_fma_f32 %0, %0, %5, %4\n\tv_fma_f32 %1, %1, %5, %4"
                 : "=&v"(s.t0), "=&v"(s.t1) : "v"(g0), "v"(g1), "v"(c1), "v"(c2));
  } else if constexpr (STG == 1) {
    asm volatile("v_mul_f32 %0, %2, %0\n\tv_mul_f32 %1, %3, %1\n\tv_exp_f32 %0, %0" : "+v"(s.t0), "+v"(s.t1) : "v"(g0), "v"(g1));
  } else if constexpr (STG == 2) {
    asm volatile("v_exp_f32 %2, %2\n\tv_mul_f32 %0, %3, %4\n\tv_mul_f32 %1, %5, %6"
                 : "=&v"(s.u0), "=&v"(s.u1), "+v"(s.t1) : "v"(g0), "v"(u0), "v"(g1), "v"(u1));
  } else if constexpr (STG == 3) {
    asm volatile("v_fma_f32 %0, %0, %2, %2\n\tv_fma_f32 %1, %1, %2, %2\n\tv_rcp_f32 %0, %0" : "+v"(s.t0), "+v"(s.t1) : "v"(d2));
  } else {
    asm volatile("v_rcp_f32 %4, %4\n\tv_mul_f32 %1, %1, %3\n\tv_mul_f32 %2, %2, %4\n\tv_cvt_pk_bf16_f32 %0, %1, %2"
                 : "=&v"(out), "+v"(s.u0), "+v"(s.u1), "+v"(s.t0), "+v"(s.t1));
  }
}

}

extern "C" __global__ __launch_bounds__(256, 1) void k_main(const u16* __restrict__ x, u16* __restrict__ out, unsigned char* __restrict__ ws) {
  __shared__ __attribute__((aligned(16))) unsigned char smem[LDS_BYTES];
  LAS unsigned char* L = (LAS unsigned char*)smem;
  const unsigned lbase = (unsigned)(size_t)L;
  int b = blockIdx.x, lane = threadIdx.x & 63, w = __builtin_amdgcn_readfirstlane(threadIdx.x >> 6);
  int h, g, j, c, T0, T;
  unsigned lane16;
#define MAP_G(b) (((b) & 6) * 2 + ((b) >> 6))
#define MAP_J(b) ((((b) >> 3) & 7) | (((b) & 1) << 3))
#define FRESH_IDS()                  \
  do {                               \
    asm volatile("" : "+v"(lane));   \
    asm volatile("" : "+s"(w));      \
    asm volatile("" : "+s"(b));      \
    h = lane >> 5;                   \
    g = MAP_G(b);                    \
    j = MAP_J(b);                    \
    c = j >> 1;                      \
    T0 = 4 * g + 2 * (j & 1);        \
    T = 4 * g + w;                   \
    lane16 = lane * 16;              \
  } while (0)
  FRESH_IDS();
  const rsrc_t rs = __builtin_amdgcn_make_buffer_rsrc((void*)ws, 0, (int)WS_BYTES, 0x00020000);
  {
    TOUCH_CODE("k_main", "k_prep", threadIdx.x, 256, lbase + R3 + 512);
    const unsigned sh = (unsigned)min(w * 15, 43) * KB;
    sfor<15>([&](auto I) { dma_piece<I>(ws + OFF_W1T + (unsigned)j * 87 * KB + sh, lbase + R1 + sh, lane16); });
  }
  {
    const int tok = 32 * (T0 + (w >> 1)) + 16 * (w & 1) + (lane & 15), gq = lane >> 4;
    const unsigned xv = (unsigned)(tok * 448 + 56 * c) * 2;
    set_m0(lbase + R4 + (unsigned)w * 2 * KB);
    asm volatile("s_nop 0");
    dma_go<0>((const unsigned char*)x, xv + 16 * gq);
    set_m0(lbase + R4 + (unsigned)w * 2 * KB + KB);
    asm volatile("s_nop 0");
    dma_go<0>((const unsigned char*)x, xv + 16 * min(gq + 4, 6));
  }

#pragma clang loop unroll(disable)
  for (int l = 0; l < 2; ++l) {
    const unsigned LB = OFF_L + (unsigned)l * L_BYTES;
    FRESH_IDS();
    v4u qf[2];
    {
      const unsigned xo = (l == 0 ? OFF_X0 : OFF_L + L_X2) + (unsigned)T * 28 * KB;
      const unsigned so = OFF_L + L_S2 + (unsigned)T * 2048;
      f32x16 ak, av, aq;
      v4u xb[29];
      LnStat ls;
      unsigned kflag = 0;
#pragma clang loop unroll(disable)
      for (int pass = 0;; ++pass) {
        asm volatile("" : "+s"(pass)::"memory");
        x_enter(rs, xo, so, l != 0, lane, xb, ls, [&]() {
          if (pass == 0) {
            VMWAIT(4 + XD);
            bar();
          }
        });
        LAS float* rb = (LAS float*)(L + R3) + w * 32;
        rb[lane & 31] = ls.r;
        kflag = ldc32(rs, (lane & 1) * 128, OFF_FLAG + (unsigned)(j * 64 + max(4 * g - 8 + 2 * w, 0)) * 128);
        gemm_k<2, true>(L + R1 + lane16, rs, xo, lane16, xb, ak, av, aq, [](auto) {});
        if (pass || !__any((ak[0] != ak[0]) | (ls.q != ls.q))) break;
        wait_valid(rs, lane16, xo, 28);
        if (l) wait_valid(rs, lane16, so, 2);
      }
      FRESH_IDS();
      LAS float* rb = (LAS float*)(L + R3) + w * 32 + 4 * h;
      f32x4 rr[4];
#pragma unroll
      for (int i = 0; i < 4; ++i) rr[i] = *(LAS f32x4*)(rb + 8 * i);
      if (l == 0) {
        const unsigned shq = (unsigned)(58 + min(w * 8, 21)) * KB;
        sfor<8>([&](auto I) { dma_piece<I>(ws + OFF_W1T + (unsigned)j * 87 * KB + shq, lbase + R1 + shq, lane16); });
      }
      v4u kv[4];
      const unsigned ko = LB + L_KV + (unsigned)((j * 64 + T) * 4) * KB + lane16;
      kv[0] = clampS4(pack8(ak, 0, ls.r));
      stc(rs, kv[0], ko);
      kv[1] = clampS4(pack8(ak, 1, ls.r));
      stc(rs, kv[1], ko + KB);
#pragma unroll
      for (int i = 0; i < 4; ++i)
#pragma unroll
        for (int e = 0; e < 4; ++e) av[4 * i + e] *= rr[i][e];
      kv[2] = clampS4(pack8n(av, 0));
      kv[3] = clampS4(pack8n(av, 1));
      if ((lane & 31) == 28) kv[2] = kv[3] = v4u{0x3F803F80u, 0x3F803F80u, 0x3F803F80u, 0x3F803F80u};
      stc(rs, kv[2], ko + 2 * KB);
      stc(rs, kv[3], ko + 3 * KB);
      LAS unsigned char* kd = opq(L + R2 + (unsigned)((8 + w) * 4) * KB + lane16);
#pragma unroll
      for (int i = 0; i < 4; ++i) lds_wr(kd + i * KB, kv[i]);
      VMWAIT(4);
      __builtin_amdgcn_s_barrier();
      LAS unsigned char* WQ = opq(L + R1 + 58 * KB + lane16);
      v4u ab[4];
#pragma unroll
      for (int q = 0; q < 4; ++q) ab[q] = lds_rd(WQ + q * KB);
      const unsigned sh = (unsigned)min(w * 15, 43) * KB;
      const unsigned char* g4 = ws + OFF_W4T + (unsigned)(j * 7) * 29 * KB + sh;
      const int tq = 4 * g - 8 + 2 * w;
      const unsigned kn = LB + L_KV + (unsigned)((j * 64 + max(tq, 0)) * 4) * KB;
      v4u nb[8];
      const PubOff pc1 = pub_off(T0, c, w, lane);
      const v4u sent4 = make_sent();
      if (l && tq >= 0 && __any(kflag == SENT)) poll32(rs, (lane & 1) * 128, OFF_FLAG + (unsigned)(j * 64 + tq) * 128);
      GAP();
      sfor<29>([&](auto K) {
        constexpr int k = K;
        mf<k == 0>(aq, ab[k & 3], xb[k]);
        if constexpr (k + 4 < 29) ab[k & 3] = lds_rd(WQ + (k + 4) * KB);
        if constexpr ((k & 1) == 0 && k < 14) dma_piece<k / 2>(g4, lbase + R1 + sh, lane16);
        if constexpr (k == 3) stc(rs, sent4, pc1.x0 + LB + L_X1);
        if constexpr (k == 7) stc(rs, sent4, pc1.x1 + LB + L_X1);
        if constexpr (k == 11) stc64(rs, v2u{sent4.x, sent4.y}, pc1.s + LB + L_X1);
        if constexpr ((k & 1) == 0 && k >= 14) nb[(k - 14) / 2] = ldc(rs, lane16 + ((k - 14) / 2) * KB, kn);
        GAP();
      });
      mfence(aq);
      qf[0] = pack8(aq, 0, ls.r);
      qf[1] = pack8(aq, 1, ls.r);
      if (h) qf[1].z = 0x00003F80u;
      asm volatile("" : "+v"(qf[0]), "+v"(qf[1]));
      if (tq >= 0) {
        unsigned m = 0;
#pragma unroll
        for (int i = 0; i < 8; ++i) m = mx4(m, nb[i]);
        if (__any(m == SENT)) {
#pragma clang loop unroll(disable)
          for (int tries = 0; tries < 64; ++tries) {
            asm volatile("" : "+v"(lane16)::"memory");
            poll32(rs, (lane & 1) * 4096 + 4092, kn);
            m = 0;
#pragma unroll
            for (int i = 0; i < 8; ++i) nb[i] = ldc(rs, lane16 + i * KB, kn);
#pragma unroll
            for (int i = 0; i < 8; ++i) m = mx4(m, nb[i]);
            if (!__any(m == SENT)) break;
          }
        }
      } else {
#pragma unroll
        for (int i = 0; i < 8; ++i) nb[i] = v4u{0, 0, 0, 0};
        nb[1].z = nb[5].z = h ? 0x0000F149u : 0u;
      }
      LAS unsigned char* kd2 = opq(L + R2 + (unsigned)(8 * w) * KB + lane16);
#pragma unroll
      for (int i = 0; i < 8; ++i) lds_wr(kd2 + i * KB, nb[i]);
    }
    FRESH_IDS();
    {
      VMWAIT(0);
      bar();
      const unsigned sh = (unsigned)(58 + min(w * 8, 21)) * KB, sh0 = (unsigned)min(w * 15, 43) * KB;
      const unsigned char* g4 = ws + OFF_W4T + (unsigned)(j * 7) * 29 * KB;

      LAS unsigned char* kvb = opq(L + R2 + (unsigned)w * 4 * KB + lane16);
      const int iq = (lane & 31) - 4 * h;
      f32x16 s[9];
      float mx0 = -INFINITY, mx1 = -INFINITY;
      v4u kf[9][2];
      v4u vf[9][2];
      sfor<2>([&](auto I) {
        constexpr int t = I * 8;
        kf[t][0] = lds_rd(kvb + (t * 4 + 0) * KB);
        kf[t][1] = lds_rd(kvb + (t * 4 + 1) * KB);
      });
      GAP();
      sfor<9>([&](auto I) {
        constexpr int i = I, t = (i == 0) ? 0 : (i == 1 ? 8 : i - 1), tn = i + 1;
        if constexpr (i == 0) mfn<true>(s[t], kf[t][0], qf[0]); else mf<true>(s[t], kf[t][0], qf[0]);
        if constexpr (tn < 8) kf[tn][0] = lds_rd(kvb + (tn * 4 + 0) * KB);
        if constexpr (i >= 4) max8<8>(mx0, mx1, s[i - 3]);
        if constexpr (i == 3) { max8<0>(mx0, mx1, s[0]); max8<8>(mx0, mx1, s[0]); }
        GAP();
        mf<false>(s[t], kf[t][1], qf[1]);
        if constexpr (tn < 8) kf[tn][1] = lds_rd(kvb + (tn * 4 + 1) * KB);
        if constexpr (i < 8) dma_piece<7 + i>(g4 + sh0, lbase + R1 + sh0, lane16);
        if constexpr (i == 2) {
          pin(s[0]);
          pin(s[8]);
#pragma unroll
          for (int r = 0; r < 16; ++r) s[0][r] = ((r & 3) + 8 * (r >> 2) <= iq) ? s[8][r] : s[0][r];
          pin(s[0]);
        }
        if constexpr (i >= 3) max8<0>(mx0, mx1, s[i - 2]);
        if constexpr (i == 7) { vf[8][0] = lds_rd(kvb + (8 * 4 + 2) * KB); vf[0][0] = lds_rd(kvb + (0 * 4 + 2) * KB); }
        if constexpr (i == 8) { vf[8][1] = lds_rd(kvb + (8 * 4 + 3) * KB); vf[0][1] = lds_rd(kvb + (0 * 4 + 3) * KB); }
        GAP();
      });
      max8<8>(mx0, mx1, s[6]);
      asm volatile("s_nop 7" : "+v"(s[7]));
      max8<0>(mx0, mx1, s[7]);
      max8<8>(mx0, mx1, s[7]);
      float m = max3f(mx0, mx1, mx1);
      m = max3f(lower32(m), upper32(m), m);
      constexpr float SC = 0.18898223650461362f * 1.4426950408889634f;
      const f32x2 sc2 = {SC, SC}, mc2 = {-m * SC, -m * SC};
      auto scale8 = [&](f32x16& v, int r0) {
#pragma unroll
        for (int r = r0; r < r0 + 8; r += 2) {
          const f32x2 z = __builtin_elementwise_fma(f32x2{v[r], v[r + 1]}, sc2, mc2);
          v[r] = z[0];
          v[r + 1] = z[1];
        }
      };
      f32x16 o;
      const unsigned char* g3 = ws + OFF_W3T + (unsigned)(c * 64 + 8 * w) * KB;
      const unsigned char* g5 = ws + OFF_W5T + (unsigned)(c * 224 + 28 * w) * KB;
      auto softmax_pv = [&](auto FIRST_LAYER) {
        constexpr bool L0 = FIRST_LAYER;
        v4u pf, pf8;
        const unsigned co = LB + L_HF + (unsigned)(((g * 2 + (w >> 1)) * 4 + (j >> 2)) * 56 + (j & 3) * 2 + (w & 1)) * KB + lane16;
        const v4u sent4 = make_sent();
        scale8(s[0], 0);
        GAP();
        sfor<17>([&](auto N) {
          constexpr int n = N, i = n >> 1, hf = n & 1, pi = (n - 1) >> 1, ph = (n - 1) & 1;
          if constexpr (n >= 1) {
            if constexpr (pi == 0) mfn<n == 1>(o, vf[8][ph], pf8);
            if constexpr (pi == 0) mf<false>(o, vf[0][ph], pf); else mfn<false>(o, vf[pi][ph], pf);
          }
          if constexpr (n < 16) {
            auto bg = [&](auto SL) {
              constexpr int sl = SL;
              if constexpr (sl == 0) {
                if constexpr ((n & 1) == 0) dma_piece<n / 2>(g4 + sh, lbase + R1 + sh, lane16);
                else if constexpr (n < 14) stc(rs, sent4, co + (n / 2) * 8 * KB);
              } else if constexpr (L0) {
                constexpr int u = 2 * n + sl - 1;
                if constexpr (u < 16) lda1<u * 4, (u & 3) * 1024>(g3 + ((u >> 3) * 32 + (u & 4)) * KB, lane16);
                else {
                  constexpr int v = u - 16, grp = v / 6, e = v % 6, jj = grp >> 1, nt = grp & 1;
                  lda1<64 + grp * 24 + e * 4, (e & 3) * 1024>(g5 + (nt * 112 + jj * 7 + (e & 4)) * KB, lane16);
                }
              }
            };
            if constexpr (i + 1 < 8) vf[i + 1][hf] = lds_rd(kvb + ((i + 1) * 4 + 2 + hf) * KB);
            bg(std::integral_constant<int, 0>{});
            GAP();
#pragma unroll
            for (int r = 8 * hf; r < 8 * hf + 5; ++r) s[i][r] = __builtin_amdgcn_exp2f(s[i][r]);
            GAP();
            bg(std::integral_constant<int, 1>{});
            GAP();
#pragma unroll
            for (int r = 8 * hf + 5; r < 8 * hf + 8; ++r) s[i][r] = __builtin_amdgcn_exp2f(s[i][r]);
            GAP();
            if constexpr (n + 1 < 16) scale8(s[(n + 1) >> 1], 8 * ((n + 1) & 1));
            GAP();
            bg(std::integral_constant<int, 2>{});
            GAP();
            if constexpr (i == 0) {
#pragma unroll
              for (int r = 8 * hf; r < 8 * hf + 8; ++r) {
                const bool d = ((r & 3) + 8 * (r >> 2) <= iq);
                s[8][r] = d ? s[0][r] : 0.f;
                s[0][r] = d ? 0.f : s[0][r];
              }
              pf8 = pack8n(s[8], hf);
            }
            pf = pack8n(s[i], hf);
          }
          GAP();
        });
      };
      if (l == 0) softmax_pv(std::true_type{}); else softmax_pv(std::false_type{});
      mfence(o);
      const float li = __builtin_amdgcn_rcpf(upper32(o[12]));
      const unsigned oo = LB + L_OF + (unsigned)(T * 32 + 2 * j) * KB + lane16;
      stc(rs, clampS4(pack8(o, 0, li)), oo);
      stc(rs, clampS4(pack8(o, 1, li)), oo + KB);
      if (l == 0) {
        sfor<16>([&](auto Q) {
          constexpr int v = 16 + Q, grp = v / 6, e = v % 6, jj = grp >> 1, nt = grp & 1;
          lda1<64 + grp * 24 + e * 4, (e & 3) * 1024>(g5 + (nt * 112 + jj * 7 + (e & 4)) * KB, lane16);
        });
      } else {
        VMWAIT(0);
      }
      bar();
    }
    FRESH_IDS();
    {
      const unsigned oo = LB + L_OF + (unsigned)(T0 * 32 + 8 * w) * KB;
      f32x16 acc[2][2];
      v4u ob[16];
      auto o_load = [&](auto N) {
        constexpr int n = N;
        ob[n] = ldc(rs, lane16 + (n & 7) * KB, oo + (n >> 3) * 32 * KB);
      };
      sfor<10>(o_load);
      LAS unsigned char* wp = opq(L + R2 + (unsigned)(w * 14) * KB + lane16);
      unsigned m = mx4(mx4(mx4(mx4(0u, ob[0]), ob[1]), ob[2]), ob[3]);
      sfor<4>([&](auto B) {
        constexpr int bt = B;
        if (__any(m == SENT)) {
#pragma clang loop unroll(disable)
          for (int tries = 0; tries < TRY_CAP; ++tries) {
            __builtin_amdgcn_s_sleep(6);
            asm volatile("" : "+v"(lane16)::"memory");
            sfor<4>([&](auto Q) { o_load(std::integral_constant<int, 4 * bt + Q>{}); });
            m = mx4(mx4(mx4(mx4(0u, ob[4 * bt]), ob[4 * bt + 1]), ob[4 * bt + 2]), ob[4 * bt + 3]);
            if (!__any(m == SENT)) break;
          }
          asm volatile("s_nop 1");
        }
        lane16 = opq(lane16);
        m = 0;
        sfor<4>([&](auto Q) {
          constexpr int n = 4 * bt + Q, tt = n >> 3, k = n & 7;
          mfa2<k == 0, k * 4, 32 + k * 4>(acc[tt][0], acc[tt][1], ob[n]);
          if constexpr (n + 10 < 16) o_load(std::integral_constant<int, n + 10>{});
          if constexpr (n + 4 < 16) m = mx4(m, ob[n + 4]);
          if constexpr (tt == 1 && k >= 1) {
            if constexpr (k == 1) { pin(acc[0][0]); pin(acc[0][1]); }
            red_write<k - 1>(wp, acc[0]);
          }
          GAP();
        });
      });
      asm volatile("s_nop 7\n\ts_nop 3" : "+v"(acc[1][0]), "+v"(acc[1][1]));
      sfor<7>([&](auto E) { red_write<E>(wp + 7 * KB, acc[1]); });
      FRESH_IDS();
      const PubOff po = pub_off(T0, c, w, lane);
      v4u res[2];
      VMWAIT(0);
      red_read(L, w, lane, res);
      publish_x(rs, LB + L_X1, po, lane, res);
      res_keep(L, w, lane, res);
      __builtin_amdgcn_s_barrier();
      const unsigned sh = (unsigned)min(w * 15, 43) * KB;
      sfor<15>([&](auto I) { dma_piece<I>(ws + OFF_W4T + (unsigned)(j * 7 + 3) * 29 * KB + sh, lbase + R2 + sh, lane16); });
    }
    FRESH_IDS();
    {
      const unsigned xo = LB + L_X1 + (unsigned)T * 28 * KB;
      const unsigned so = LB + L_S1 + (unsigned)T * 2048;
      v4u hb[7][4][2];
      const unsigned hi = LB + L_HF + (unsigned)(((g * 2 + (j & 1)) * 4 + w) * 56) * KB;
      auto h_load = [&](auto N) {
        constexpr int n = N, i = n >> 3, jj = (n >> 1) & 3, tt = n & 1;
        hb[i][jj][tt] = ldc(rs, lane16 + n * KB, hi);
      };
      {
        v4u xb[29];
        f32x16 a[7];
        LnStat ls;
#pragma clang loop unroll(disable)
        for (int pass = 0;; ++pass) {
          asm volatile("" : "+s"(pass)::"memory");
          x_enter(rs, xo, so, true, lane, xb, ls, [&]() {});
          const PubOff pc2 = pub_off(T0, c, w, lane);
          const unsigned x2o = l ? OOB : OFF_L + L_X2;
          v4u sent4;
          gemm_k<3, false>(L + R1 + lane16, rs, xo, lane16, xb, a[0], a[1], a[2], [&](auto K) {
            constexpr int k = K;
            if constexpr (k == 19) sent4 = make_sent();
            if constexpr (k == 20) stc(rs, sent4, (pc2.x0 + x2o) | (pc2.x0 & OOB));
            if constexpr (k == 23) stc(rs, sent4, (pc2.x1 + x2o) | (pc2.x1 & OOB));
            if constexpr (k == 26) stc64(rs, v2u{sent4.x, sent4.y}, (pc2.s + x2o) | (pc2.s & OOB));
          });
          if (pass || !__any((a[0][0] != a[0][0]) | (ls.q != ls.q))) break;
          wait_valid(rs, lane16, xo, 28);
          wait_valid(rs, lane16, so, 2);
        }
        __builtin_amdgcn_s_barrier();
        const unsigned ho = LB + L_HF + (unsigned)(((g * 2 + (w >> 1)) * 4 + (j >> 2)) * 56 + (j & 3) * 2 + (w & 1)) * KB + lane16;
        const unsigned sh = (unsigned)min(w * 15, 43) * KB;
        const unsigned char* g4 = ws + OFF_W4T + (unsigned)(j * 7 + 5) * 29 * KB + sh;
        const unsigned char* g5 = ws + OFF_W5T + (unsigned)(c * 224 + 28 * w) * KB;
        constexpr float K1 = -2.f * 0.7978845608028654f * 1.4426950408889634f, K2 = K1 * 0.044715f;
        float c1 = K1 * ls.r, c2 = K2 * ls.r * ls.r * ls.r, d2 = ls.den * ls.den;
        asm volatile("" : "+v"(c1), "+v"(c2), "+v"(d2));
        GeluSt gs;
        v4u hv, sent4;
        const unsigned nk = (l ? OOB : OFF_L + L_BYTES + L_KV + (unsigned)((j * 64 + T) * 4) * KB) + lane16;
        const unsigned no = (l ? OOB : OFF_L + L_BYTES + L_OF + (unsigned)(T * 32 + 2 * j) * KB) + lane16;
        v4u ab[6];
        LAS unsigned char* WA = opq(L + R2 + lane16);
        LAS unsigned char* WB = opq(L + R1 + lane16);
        lane16 = opq(lane16);
        auto a_addr = [&](int q) -> LAS unsigned char* { return q < 58 ? WA + q * KB : WB + (q - 58) * KB; };
#pragma unroll
        for (int q = 0; q < 6; ++q) ab[q] = lds_rd(a_addr(q));
        GAP();
        sfor<140>([&](auto Q) {
          constexpr int q = Q;
          constexpr bool dma = q < 30 && q % 2 == 0;
          if constexpr (q < 116) {
            constexpr int t = 3 + q / 29, k = q % 29;
            if constexpr ((q & 1) == 0) use(ab[(q + 1) % 6]);
            if constexpr (dma) set_m0(lbase + R1 + sh + ((q / 2) & ~3) * KB);
            mf<k == 0>(a[t], ab[q % 6], xb[k]);
            if constexpr (dma) dma_go<((q / 2) & 3) * 1024>(g4 + ((q / 2) & ~3) * KB, lane16);
            if constexpr (q == 52) {
              VMWAIT(4);
              bar();
            }
            if constexpr (q + 6 < 116) ab[q % 6] = lds_rd(a_addr(q + 6));
            if constexpr (q == 31) sent4 = make_sent();
            if constexpr (q >= 33 && q < 81 && (q - 33) % 8 == 0) {
              constexpr int z = (q - 33) / 8;
              if constexpr (z < 4) stc(rs, sent4, nk + z * KB); else stc(rs, sent4, no + (z - 4) * KB);
            }
            if constexpr (q >= 88 && (q & 1) == 0) h_load(std::integral_constant<int, (q - 88) / 2>{});
            if constexpr (q >= 54 && q < 86 && (q & 3) == 2) {
              if (l == 0) {
                sfor<2>([&](auto Z) {
                  constexpr int v = 32 + (q - 54) / 2 + Z, grp = v / 6, e = v % 6, jj = grp >> 1, nt = grp & 1;
                  lda1<64 + grp * 24 + e * 4, (e & 3) * 1024>(g5 + (nt * 112 + jj * 7 + (e & 4)) * KB, lane16);
                });
              }
            }
          }
          if constexpr (q == 116) __builtin_amdgcn_s_barrier();
          constexpr int pr = q / 5, stg = q % 5, t = pr >> 2, pp = pr & 3;
          if constexpr (stg == 0 && pp == 0) pin(a[t]);
          unsigned ov = 0;
          gelu_stage<stg>(gs, a[t][2 * pp], a[t][8 + 2 * pp], a[t][2 * pp + 1], a[t][8 + 2 * pp + 1], c1, c2, d2, ov);
          if constexpr (stg == 4) {
            hv[pp] = ov;
            if constexpr (pp == 3) stc(rs, hv, ho + t * 8 * KB);
          }
          if constexpr (q >= 116 && q < 140 && (q % 3) == 2) h_load(std::integral_constant<int, 14 + (q - 116) / 3>{});
          GAP();
        });
      }
      FRESH_IDS();

      f32x16 acc[2][2];
      v4u wl[4][2];
      LAS unsigned char* wp = opq(L + R2 + (unsigned)(w * 14) * KB + lane16);
      const unsigned sh1 = (unsigned)min(w * 22, 65) * KB;
      const unsigned char* g1 = ws + OFF_W1T + (unsigned)j * 87 * KB + sh1;
      unsigned m = 0;
#pragma unroll
      for (int jj = 0; jj < 4; ++jj) m = mx4(mx4(m, hb[0][jj][0]), hb[0][jj][1]);
      sfor<7>([&](auto I) {
        constexpr int i = I;
        if (__any(m == SENT)) {
#pragma clang loop unroll(disable)
          for (int tries = 0; tries < TRY_CAP; ++tries) {
            __builtin_amdgcn_s_sleep(8);
            asm volatile("" : "+v"(lane16)::"memory");
            m = 0;
            sfor<8>([&](auto Q) { h_load(std::integral_constant<int, 8 * i + Q>{}); });
#pragma unroll
            for (int jj = 0; jj < 4; ++jj) m = mx4(mx4(m, hb[i][jj][0]), hb[i][jj][1]);
            if (!__any(m == SENT)) break;
          }
          asm volatile("s_nop 1");
        }
        lane16 = opq(lane16);
        m = 0;
        sfor<8>([&](auto Q) {
          if constexpr (i == 6) {
            constexpr int tt = Q >> 2, jj = Q & 3;
            mf2(acc[tt][0], acc[tt][1], wl[jj][0], wl[jj][1], hb[i][jj][tt]);
            if constexpr (Q == 5) { pin(acc[0][0]); pin(acc[0][1]); }
            if constexpr (Q >= 5) { red_write<2 * (Q - 5)>(wp, acc[0]); red_write<2 * (Q - 5) + 1>(wp, acc[0]); }
          } else {
            constexpr int jj = Q >> 1, tt = Q & 1, n = 22 + 8 * i + Q;
            mfa2<(i == 0 && jj == 0), 64 + (jj * 2) * 24 + i * 4, 64 + (jj * 2 + 1) * 24 + i * 4>(acc[tt][0], acc[tt][1], hb[i][jj][tt]);
            if constexpr (n < 56) h_load(std::integral_constant<int, n>{});
            else if constexpr (n < 64) {
              constexpr int z = n - 56;
              wl[z >> 1][z & 1] = ldp(rs, lane16 + ((z >> 1) * 7 + 6) * KB, OFF_W5T + (unsigned)(c * 224 + 28 * w + (z & 1) * 112) * KB);
            }
            m = mx4(m, hb[i + 1][jj][tt]);
          }
          GAP();
        });
      });
      red_write<6>(wp, acc[0]);
      asm volatile("s_nop 7\n\ts_nop 3" : "+v"(acc[1][0]), "+v"(acc[1][1]));
      sfor<7>([&](auto E) { red_write<E>(wp + 7 * KB, acc[1]); });
      FRESH_IDS();
      v4u res[2];
      VMWAIT(0);
      if (l == 0) {
        sfor<8>([&](auto I) { dma_piece<I>(g1, lbase + R1 + sh1, lane16); });
        const PubOff po = pub_off(T0, c, w, lane);
        red_read(L, w, lane, res);
        publish_x(rs, OFF_L + L_X2, po, lane, res);
        if (lane == 0) __builtin_amdgcn_raw_buffer_store_b32(0u, rs, OFF_FLAG + (unsigned)(j * 64 + T) * 128, 0, AUX_SC1);
        res_keep(L, w, lane, res);
        sfor<14>([&](auto I) { dma_piece<8 + I>(g1, lbase + R1 + sh1, lane16); });
      } else {
        const int tok = 32 * (T0 + (w >> 1)) + 16 * (w & 1) + (lane & 15), gq = lane >> 4;
        u16* p = out + tok * 448 + 56 * c;
        red_read(L, w, lane, res);
        *(v4u*)(p + 8 * gq) = res[0];
        if (gq < 3) *(v4u*)(p + 8 * (gq + 4)) = res[1];
      }
    }
  }
}

template <int CTRL> DI float dpp_add(float v) {
  return v + __builtin_bit_cast(float, __builtin_amdgcn_update_dpp(0, __builtin_bit_cast(int, v), CTRL, 0xf, 0xf, true));
}
DI float sum32(float v) {
  v = dpp_add<0xB1>(v);
  v = dpp_add<0x4E>(v);
  v = dpp_add<0x141>(v);
  v = dpp_add<0x140>(v);
  return add16(v);
}
extern "C" __global__ __launch_bounds__(256, 2) void k_prep(const u16* __restrict__ x, const u16* __restrict__ wqkv, const u16* __restrict__ wo,
                                                           const u16* __restrict__ wg, const u16* __restrict__ wu, const u16* __restrict__ wd,
                                                           unsigned char* __restrict__ ws) {
  __shared__ __attribute__((aligned(16))) unsigned char smem[116 * 256 + 256];
  LAS unsigned char* img = (LAS unsigned char*)smem;
  const unsigned ibase = (unsigned)(size_t)img, sink = ibase + 116 * 256;
  const int role = (blockIdx.x >> 3) & 1, b = (blockIdx.x & 7) | ((blockIdx.x >> 4) << 3);
  const int tid = threadIdx.x, lane = tid & 63, w = __builtin_amdgcn_readfirstlane(tid >> 6);
  const int h = lane >> 5;
  const rsrc_t rs = __builtin_amdgcn_make_buffer_rsrc((void*)ws, 0, (int)WS_BYTES, 0x00020000);
  {
    TOUCH_CODE("k_prep", "k_tail", tid, 256, sink);
  }
  asm volatile("" ::"s"(x), "s"(wqkv), "s"(wo), "s"(wg), "s"(wu), "s"(wd), "s"(ws));
  if (role == 0) {
    const int kind = (b >= 48) + (b >= 160) + (b >= 224);
    const int part = b >> 4, i2 = b - 160;
    const int rq = kind == 2 ? i2 >> 2 : b & 3;
    const int cg = kind == 1 ? (b - 48) >> 2 : (kind == 2 ? i2 & 3 : (b >> 2) & 3);
    const int stride = kind == 0 ? 1344 : (kind == 1 ? 1792 : 448);
    const u16* pA = (kind == 0 ? wqkv + (part == 0 ? 448 : (part == 1 ? 896 : 0)) : (kind == 1 ? wg : (kind == 2 ? wd : wo))) + (kind == 1 ? 64 : 112) * cg;
    const u16* pB = kind == 1 ? wu + 64 * cg : pA + 64;
    const int ti = 4 * cg + w;
    const unsigned dst = kind == 0 ? OFF_W1T + (unsigned)((ti * 3 + part) * 29) * KB
                                   : (kind == 1 ? OFF_W4T + (unsigned)(ti * 29) * KB : (kind == 2 ? OFF_W5T + (unsigned)(ti * 112) * KB : OFF_W3T + (unsigned)(ti * 32) * KB));
    {
      const int r4 = lane >> 4, ch = (lane & 15) ^ (r4 << 2);
      const int chc = (kind == 1) ? ch : min(ch, 13);
      const u16* src = (chc < 8 ? pA : pB) + 8 * (chc & 7) + (112 * rq + 28 * w + r4) * stride;
#pragma unroll
      for (int i = 0; i < 7; ++i) {
        asm volatile("s_mov_b32 m0, %1\n\ts_nop 0\n\tglobal_load_lds_dwordx4 %0, off" ::"v"(src + 4 * i * stride), "s"(ibase + (unsigned)(28 * w + 4 * i) * 256)
                     : "memory");
      }
    }
    touch(ws + dst, 128, lane, 64, sink);
    const int un = ((lane >> 4) & 1) * 4 + (lane & 3), q = (lane & 15) >> 2;
    int col;
    bool valid = true;
    col = kind == 0 ? 28 * w + 4 * min(un, 6) : (kind == 1 ? (un < 4 ? 0 : 64) + 16 * w + 4 * (un & 3) : min(56 * (w >> 1) + 32 * (w & 1) + 4 * un, 108));
    valid = (lane & 31) < (kind == 0 ? 28 : ((kind >= 2 && (w & 1)) ? 24 : 32));
    const int dlo = (kind < 2) ? 8 * h : 4 * h, dhi = (kind < 2) ? 4 : 8;
    LAS unsigned char* base = img + (dlo + q) * 256 + (((col >> 3) ^ (q << 2)) << 4) + (col & 4) * 2;
    const int nfr = (kind == 3) ? 8 : 7;
    const unsigned keep = valid ? 0xFFFFFFFFu : 0u;
    const unsigned dv = dst + (unsigned)(nfr * rq) * KB + lane * 16;
    f32x16 cs;
#pragma unroll
    for (int i = 0; i < 16; ++i) cs[i] = 0.f;
    const v4u ones = {0x3F803F80u, 0x3F803F80u, 0x3F803F80u, 0x3F803F80u};
    VMWAIT(0);
    __builtin_amdgcn_s_barrier();
    if (b < 240) {
#pragma unroll
      for (int f = 0; f < 8; ++f) {
        if (f < nfr) {
          const int rb = (kind == 3) ? 28 * (f >> 1) + 16 * (f & 1) : 16 * f;
          s16x4 lo = __builtin_amdgcn_ds_read_tr16_b64_v4i16((LAS s16x4*)(base + rb * 256));
          s16x4 hi = __builtin_amdgcn_ds_read_tr16_b64_v4i16((LAS s16x4*)(base + (rb + dhi) * 256));
          const v2u l2 = __builtin_bit_cast(v2u, lo), h2 = __builtin_bit_cast(v2u, hi);
          const unsigned kh = (kind == 3 && (f & 1) && h) ? 0u : keep;
          const v4u fr = {l2.x & keep, l2.y & keep, h2.x & kh, h2.y & kh};
          stc(rs, fr, dv + f * KB);
          cs = __builtin_amdgcn_mfma_f32_32x32x16_bf16(__builtin_bit_cast(bf16x8, ones), __builtin_bit_cast(bf16x8, fr), cs, 0, 0, 0);
        }
      }
      if (kind < 2 && !h) {
        const float c = cs[0], c0 = rbf(c), c1 = rbf(c - c0), c2 = rbf(c - c0 - c1);
        stc64(rs, v2u{pk(c0, c1), pk(c2, 0.f)}, dst + 28 * KB + (unsigned)((rq >> 1) * 512 + lane * 16 + (rq & 1) * 8));
      }
    }
  } else {
    const int row = 8 * b + 2 * w + h, li = lane & 31;
    const v4u* p = (const v4u*)(x + row * 448 + min(li, 27) * 16);
    const v4u xa = p[0], xb = p[1];
    GAP();
    {
      if (w == 3 && b < 8) TOUCH_CODE("k_main", "k_prep", b * 64 + lane, 512, sink);
    }
    {
      const v4u s4 = {SENT, SENT, SENT, SENT};
      constexpr int PER = PREP_FILL / (1024 * KB);
#pragma unroll
      for (int i = 0; i < PER; ++i) stc(rs, s4, OFF_L + (unsigned)((b * 4 + w) * PER + i) * KB + lane * 16);
      stc(rs, s4, lane < 8 ? OFF_FLAG + (unsigned)(b * 4 + w) * 128 + lane * 16 : OOB);
    }
    GAP();
    float v[16];
    const unsigned d[8] = {xa.x, xa.y, xa.z, xa.w, xb.x, xb.y, xb.z, xb.w};
#pragma unroll
    for (int i = 0; i < 8; ++i) { v[2 * i] = bflo(d[i]); v[2 * i + 1] = bfhi(d[i]); }
    float s = 0.f;
#pragma unroll
    for (int i = 0; i < 16; ++i) s += v[i];
    s = sum32(li < 28 ? s : 0.f);
    const float mean = s / 448.f;
    float q = 0.f;
#pragma unroll
    for (int i = 0; i < 16; ++i) { const float e = v[i] - mean; q += e * e; }
    q = sum32(li < 28 ? q : 0.f);
    const float mb = rbf(mean), den = rbf(sqrtf(rbf(rbf(q / 448.f) + 1e-5f)));
    const float r = 1.f / den;
    unsigned o8[8];
#pragma unroll
    for (int i = 0; i < 8; ++i) {
      const unsigned dd = pk(v[2 * i] - mb, v[2 * i + 1] - mb);
      const float d0 = bflo(dd), d1 = bfhi(dd);
      const float q0 = d0 * r, q1 = d1 * r;
      o8[i] = pk(fmaf(fmaf(-q0, den, d0), r, q0), fmaf(fmaf(-q1, den, d1), r, q1));
    }
    if (li < 28) {
      unsigned char* po = ws + OFF_X0 + (unsigned)((row >> 5) * 28 + li) * KB + (row & 31) * 16;
      *(v4u*)po = v4u{o8[0], o8[1], o8[2], o8[3]};
      *(v4u*)(po + 512) = v4u{o8[4], o8[5], o8[6], o8[7]};
    }
  }
}

extern "C" __global__ void k_tail() {}

extern "C" void kernel_launch(void* const* d_in, const int* in_sizes, int n_in, void* d_out, int out_size, void* d_ws, size_t ws_size,
                              hipStream_t stream) {
  const u16* x = (const u16*)d_in[0];
  const u16* wqkv = (const u16*)d_in[1];
  const u16* wo = (const u16*)d_in[2];
  const u16* wg = (const u16*)d_in[4];
  const u16* wu = (const u16*)d_in[5];
  const u16* wd = (const u16*)d_in[6];
  unsigned char* ws = (unsigned char*)d_ws;
  k_prep<<<512, 256, 0, stream>>>(x, wqkv, wo, wg, wu, wd, ws);
  k_main<<<256, 256, 0, stream>>>(x, (u16*)d_out, ws);
}
```
